```python
import torch

def _softmax(x, axis=-1):
    return torch.softmax(x, dim=axis)
def _gelu(x):
    return torch.nn.functional.gelu(x, approximate="tanh")
def _silu(x):
    return torch.nn.functional.silu(x)
def _sigmoid(x):
    return torch.sigmoid(x)
def _layernorm(x, axis=-1, eps=1e-5):
    m = x.mean(dim=axis, keepdim=True)
    v = x.var(dim=axis, keepdim=True, unbiased=False)
    return (x - m) / torch.sqrt(v + eps)
def _scatter_add(tgt, idx, src, axis=0):
    return tgt.clone().index_add_(axis, idx, src)
def _segment_sum(src, seg, n):
    out = torch.zeros((n,) + src.shape[1:], dtype=src.dtype)
    return out.index_add_(0, seg, src)
def _pad(x, pw):
    flat = tuple(v for pair in reversed(pw) for v in pair)
    return torch.nn.functional.pad(x, flat)

kernel_name = 'gated_mix__sliding_local_dilated__mixer_s_mlp__prompt'

def setup_inputs():

    _w = (lambda a: (lambda t: t.to(torch.bfloat16) if t.is_floating_point() else t)(torch.as_tensor(a)))
    def _A_setup():
        B, S, D = 1, 2048, 640
        return {
            "x": _w((lambda *s: torch.randn(*s, dtype=torch.bfloat16) * 0.05)(B, S, D)),
            "wqkv": _w((lambda *s: torch.randn(*s, dtype=torch.bfloat16) * 3.2)(D, 3 * D)),
            "wo": _w((lambda *s: torch.randn(*s, dtype=torch.bfloat16) * 0.1)(D, D)),
        }
    _A_in = _A_setup()

    def _B_setup():
        B, S, D, F = 1, 2048, 640, 1728
        return {
            "x":  _w((lambda *s: torch.randn(*s, dtype=torch.bfloat16) * 0.05)(B, S, D)),
            "w1": _w((lambda *s: torch.randn(*s, dtype=torch.bfloat16) * 3.2)(D, D) * (D ** -0.5)),
            "w2": _w((lambda *s: torch.randn(*s, dtype=torch.bfloat16) * 3.2)(D, D) * (D ** -0.5)),
            "w3": _w((lambda *s: torch.randn(*s, dtype=torch.bfloat16) * 3.2)(D, F) * (D ** -0.5)),
            "w4": _w((lambda *s: torch.randn(*s, dtype=torch.bfloat16) * 3.2)(F, D) * (F ** -0.5)),
        }
    _B_in = _B_setup()

    return {
        "A_x": _A_in["x"],
        "A_wqkv": _A_in["wqkv"],
        "A_wo": _A_in["wo"],
        "B_x": _B_in["x"],
        "B_w1": _B_in["w1"],
        "B_w2": _B_in["w2"],
        "B_w3": _B_in["w3"],
        "B_w4": _B_in["w4"],
        "gate_w": _w((lambda *s: torch.randn(*s, dtype=torch.bfloat16) * 0.05)(640) * 16.0),
    }

def reference(A_x, A_wqkv, A_wo, B_x, B_w1, B_w2, B_w3, B_w4, gate_w):
    def _A_ref(x, wqkv, wo):
        B, S, D = x.shape
        H = 32; Dh = D // H
        W, dil = 8, 2
        qkv = (lambda x, *d: x.permute(*d))((x @ wqkv).reshape(B, S, 3, H, Dh), 2, 0, 3, 1, 4)
        q, k, v = qkv[0], qkv[1], qkv[2]
        scale = Dh ** -0.5
        pos = torch.arange(S)
        off = torch.arange(W) * dil
        idx = pos[:, None] - off[None, :]
        mask = idx >= 0
        idx_c = torch.where(mask, idx, 0).reshape(-1)
        kg = k[:, :, idx_c, :].reshape(B, H, S, W, Dh)
        vg = v[:, :, idx_c, :].reshape(B, H, S, W, Dh)
        logits = torch.einsum("bhsd,bhswd->bhsw", q, kg) * scale
        logits = torch.where(mask[None, None, :, :], logits, -1e9)
        attn = _softmax(logits, axis=-1)
        o = torch.einsum("bhsw,bhswd->bhsd", attn, vg)
        return (lambda x, *d: x.permute(*d))(o, 0, 2, 1, 3).reshape(B, S, D) @ wo

    def _B_ref(x, w1, w2, w3, w4):
        B, S, D = x.shape
        h = _gelu(_layernorm(x, axis=-1) @ w1)
        c = D // 4
        g0 = _pad(h[:, :, 0*c:1*c], ((0, 0), (1, 0), (0, 0)))[:, :S, :]
        g1 = _pad(h[:, :, 1*c:2*c], ((0, 0), (0, 1), (0, 0)))[:, 1:, :]
        g2 = _pad(h[:, :, 2*c:3*c], ((0, 0), (2, 0), (0, 0)))[:, :S, :]
        g3 = _pad(h[:, :, 3*c:   ], ((0, 0), (0, 2), (0, 0)))[:, 2:, :]
        hs = torch.concatenate([g0, g1, g2, g3], axis=-1) @ w2
        x1 = x + hs
        h2 = _gelu(_layernorm(x1, axis=-1) @ w3) @ w4
        return x1 + h2

    x = A_x
    g = _sigmoid((x * gate_w).sum(-1, keepdims=True))
    a = _A_ref(x, A_wqkv, A_wo)
    b = _B_ref(x, B_w1, B_w2, B_w3, B_w4)
    return g * a + (1 - g) * b
```

```cpp
#include <hip/hip_runtime.h>
#include <hip/hip_bf16.h>
#include <stdint.h>

typedef unsigned short u16;
__device__ __forceinline__ float bf2f(u16 b) { return __uint_as_float(((unsigned)b) << 16); }
__device__ __forceinline__ u16 f2bf(float f) { unsigned u = __float_as_uint(f); u += 0x7FFFu + ((u >> 16) & 1u); return (u16)(u >> 16); }
__device__ __forceinline__ float gelu_t(float x) { float u = 0.7978845608028654f * (x + 0.044715f * x * x * x); return 0.5f * x * (1.f + tanhf(u)); }

constexpr int S = 2048, D = 640, F = 1728, H = 32, DH = 20;

__global__ void k_ln(const u16* x, u16* xn, const u16* gw, float* g) {
  int row = blockIdx.x; int t = threadIdx.x;
  float v[10]; float s = 0, gs = 0;
  for (int i = 0; i < 10; ++i) { v[i] = bf2f(x[row * D + t + 64 * i]); s += v[i]; if (gw) gs += v[i] * bf2f(gw[t + 64 * i]); }
  for (int o = 32; o; o >>= 1) { s += __shfl_xor(s, o); gs += __shfl_xor(gs, o); }
  float m = s / D; float q = 0;
  for (int i = 0; i < 10; ++i) { float d = v[i] - m; q += d * d; }
  for (int o = 32; o; o >>= 1) q += __shfl_xor(q, o);
  float r = rsqrtf(q / D + 1e-5f);
  for (int i = 0; i < 10; ++i) xn[row * D + t + 64 * i] = f2bf((v[i] - m) * r);
  if (gw && t == 0) g[row] = 1.f / (1.f + __expf(-gs));
}

template <int MODE>
__global__ void k_gemm(const u16* A, const u16* B, u16* C, int N, int K, const u16* res, const u16* a_br, const float* g) {
  int col = blockIdx.x * blockDim.x + threadIdx.x; int row = blockIdx.y;
  if (col >= N) return;
  float acc = 0;
  for (int k = 0; k < K; ++k) {
    int r = row;
    if (MODE == 2) { int grp = k / 160; r = grp == 0 ? row - 1 : grp == 1 ? row + 1 : grp == 2 ? row - 2 : row + 2; }
    float a = (r >= 0 && r < S) ? bf2f(A[r * K + k]) : 0.f;
    acc += a * bf2f(B[k * N + col]);
  }
  if (MODE == 1) acc = gelu_t(acc);
  if (MODE == 2) acc = bf2f(res[row * N + col]) + acc;
  if (MODE == 3) { float b = bf2f(res[row * N + col]) + acc; float gg = g[row]; acc = gg * bf2f(a_br[row * N + col]) + (1.f - gg) * b; }
  C[row * N + col] = f2bf(acc);
}

__global__ void k_attn(const u16* qkv, u16* o) {
  int idx = blockIdx.x * blockDim.x + threadIdx.x; int s = idx / H, h = idx % H;
  float q[DH]; for (int d = 0; d < DH; ++d) q[d] = bf2f(qkv[s * 1920 + h * DH + d]);
  float l[8]; float mx = -1e30f;
  for (int w = 0; w < 8; ++w) { int p = s - 2 * w; float a = -1e9f; if (p >= 0) { a = 0; for (int d = 0; d < DH; ++d) a += q[d] * bf2f(qkv[p * 1920 + 640 + h * DH + d]); a *= 0.22360679774997896f; } l[w] = a; mx = fmaxf(mx, a); }
  float sum = 0; for (int w = 0; w < 8; ++w) { l[w] = __expf(l[w] - mx); sum += l[w]; }
  float inv = 1.f / sum;
  for (int d = 0; d < DH; ++d) { float a = 0; for (int w = 0; w < 8; ++w) { int p = s - 2 * w; if (p < 0) p = 0; a += l[w] * bf2f(qkv[p * 1920 + 1280 + h * DH + d]); } o[s * D + h * DH + d] = f2bf(a * inv); }
}

extern "C" void kernel_launch(void* const* d_in, const int* in_sizes, int n_in, void* d_out, int out_size, void* d_ws, size_t ws_size, hipStream_t stream) {
  const u16* x = (const u16*)d_in[0]; const u16* wqkv = (const u16*)d_in[1]; const u16* wo = (const u16*)d_in[2];
  const u16* w1 = (const u16*)d_in[4]; const u16* w2 = (const u16*)d_in[5]; const u16* w3 = (const u16*)d_in[6]; const u16* w4 = (const u16*)d_in[7]; const u16* gw = (const u16*)d_in[8];
  char* ws = (char*)d_ws;
  u16* xn = (u16*)ws; ws += S * D * 2; u16* qkv = (u16*)ws; ws += S * 1920 * 2; u16* h = (u16*)ws; ws += S * D * 2; u16* o = (u16*)ws; ws += S * D * 2;
  u16* a = (u16*)ws; ws += S * D * 2; u16* x1 = (u16*)ws; ws += S * D * 2; u16* x1n = (u16*)ws; ws += S * D * 2; u16* u = (u16*)ws; ws += S * F * 2; float* g = (float*)ws;
  k_ln<<<S, 64, 0, stream>>>(x, xn, gw, g);
  k_gemm<0><<<dim3(1920 / 64, S), 64, 0, stream>>>(x, wqkv, qkv, 1920, D, nullptr, nullptr, nullptr);
  k_gemm<1><<<dim3(D / 64, S), 64, 0, stream>>>(xn, w1, h, D, D, nullptr, nullptr, nullptr);
  k_attn<<<S * H / 256, 256, 0, stream>>>(qkv, o);
  k_gemm<0><<<dim3(D / 64, S), 64, 0, stream>>>(o, wo, a, D, D, nullptr, nullptr, nullptr);
  k_gemm<2><<<dim3(D / 64, S), 64, 0, stream>>>(h, w2, x1, D, D, x, nullptr, nullptr);
  k_ln<<<S, 64, 0, stream>>>(x1, x1n, nullptr, nullptr);
  k_gemm<1><<<dim3(F / 64, S), 64, 0, stream>>>(x1n, w3, u, F, D, nullptr, nullptr, nullptr);
  k_gemm<3><<<dim3(D / 64, S), 64, 0, stream>>>(u, w4, (u16*)d_out, D, F, x1, a, g);
}
```

```cpp
#include <hip/hip_runtime.h>
#include <stdint.h>

typedef unsigned short u16;
typedef short bf16x8 __attribute__((ext_vector_type(8)));
typedef short bf16x4 __attribute__((ext_vector_type(4)));
typedef float f32x4 __attribute__((ext_vector_type(4)));
typedef float f32x2 __attribute__((ext_vector_type(2)));
typedef __bf16 bfv2 __attribute__((ext_vector_type(2)));
typedef unsigned v4u __attribute__((ext_vector_type(4)));
typedef unsigned v2u __attribute__((ext_vector_type(2)));
#define LAS __attribute__((address_space(3)))
#define DEV __device__ __forceinline__

constexpr int S = 2048, D = 640, F = 1728;
constexpr size_t KB = 1024;
constexpr size_t WS_W2 = 0;
constexpr size_t WS_WO = WS_W2 + 800 * KB;
constexpr size_t WS_W3 = WS_WO + 800 * KB;
constexpr size_t WS_W4 = WS_W3 + 2160 * KB;
constexpr size_t WS_QKV = WS_W4 + 2160 * KB;
constexpr size_t WS_HS = WS_QKV + 7680 * KB;
constexpr size_t WS_O = WS_HS + 2560 * KB;
constexpr size_t WS_X1 = WS_O + 2560 * KB;
constexpr size_t WS_U = WS_X1 + 2560 * KB;
constexpr size_t WS_ST = WS_U + 6912 * KB;
constexpr size_t WS_A = WS_ST + 512 * KB;
constexpr size_t WS_END = WS_A + 256 * 24 * KB;

DEV f32x4 mfma(bf16x8 a, bf16x8 b, f32x4 c) { return __builtin_amdgcn_mfma_f32_16x16x32_bf16(a, b, c, 0, 0, 0); }
DEV unsigned pk2(float a, float b) { f32x2 v = {a, b}; bfv2 r = __builtin_convertvector(v, bfv2); return __builtin_bit_cast(unsigned, r); }
DEV float bflo(unsigned d) { return __uint_as_float(d << 16); }
DEV float bfhi(unsigned d) { return __uint_as_float(d & 0xffff0000u); }
DEV float gelu_t(float x) {
  const float c1 = -2.0f * 0.7978845608028654f * 1.4426950408889634f;
  const float c2 = c1 * 0.044715f;
  float t = x * __builtin_fmaf(x * x, c2, c1);
  float e = __builtin_amdgcn_exp2f(t);
  return x * __builtin_amdgcn_rcpf(1.0f + e);
}
DEV void glds16(const void* g, LAS char* l) {
  __builtin_amdgcn_global_load_lds((const __attribute__((address_space(1))) unsigned*)g, (LAS unsigned*)l, 16, 0, 0);
}
DEV bf16x4 trread(LAS char* p) { return __builtin_amdgcn_ds_read_tr16_b64_v4i16((LAS bf16x4*)p); }
DEV bf16x8 cat(bf16x4 a, bf16x4 b) { return __builtin_shufflevector(a, b, 0, 1, 2, 3, 4, 5, 6, 7); }
#define WAITV(n) asm volatile("s_waitcnt vmcnt(%0)" ::"n"(n) : "memory")
#define WAITL0() asm volatile("s_waitcnt lgkmcnt(0)" ::: "memory")
#define BAR() __builtin_amdgcn_s_barrier()

struct Params {
  const u16 *x, *wqkv, *wo, *w1, *w2, *w3, *w4, *gw;
  u16* out;
  char* ws;
};

template <bool ISW1>
DEV void s1_main(const Params& p, LAS char* L, int rt128, int c, int w, int lane) {
  const int wm = w & 3, wn = w >> 2;
  const int cc = ISW1 ? c - 12 : c;
  const int pitch = ISW1 ? 1280 : 3840;
  const char* Wb = (const char*)(ISW1 ? p.w1 : p.wqkv) + ((320 * cc) & ~127);
  const int U0 = ((320 * cc) & 127) >> 5;
  const int srow = lane >> 3, ss = lane & 7;
  const char* xs0; const char* xs1; const char* wsrc;
  {
    int r0 = 8 * w + srow, r1 = 64 + 8 * w + srow;
    xs0 = (const char*)p.x + (size_t)(rt128 * 128 + r0) * 1280 + ((ss ^ ((r0 >> 1) & 7)) << 4);
    xs1 = (const char*)p.x + (size_t)(rt128 * 128 + r1) * 1280 + ((ss ^ ((r1 >> 1) & 7)) << 4);
    int kr = 8 * w + srow;
    int swz = ((kr >> 1) & 1) | (((kr >> 3) & 1) << 1);
    wsrc = Wb + (size_t)kr * pitch + (((((ss >> 1) ^ swz) << 1) | (ss & 1)) << 4);
  }
  auto stage = [&](int ch, int slot) {
    LAS char* base = L + slot * 40960;
    glds16(xs0 + ch * 128, base + w * 1024);
    glds16(xs1 + ch * 128, base + 8192 + w * 1024);
    const char* ws_ = wsrc + (size_t)ch * 64 * pitch;
    glds16(ws_, base + 16384 + w * 1024);
    glds16(ws_ + 128, base + 16384 + 8192 + w * 1024);
    glds16(ws_ + 256, base + 16384 + 16384 + w * 1024);
  };
  const int r = lane & 15, g = lane >> 4;
  int xoff[2];
  xoff[0] = wm * 4096 + r * 128 + ((g ^ (r >> 1)) << 4);
  xoff[1] = wm * 4096 + r * 128 + (((4 + g) ^ (r >> 1)) << 4);
  int woff[5];
  {
    int q = (lane >> 2) & 3, pp = lane & 3;
    int swz = ((q >> 1) & 1) | ((g & 1) << 1);
#pragma unroll
    for (int tau = 0; tau < 5; ++tau) {
      int t = 5 * wn + (tau + wn) % 5;
      int U = U0 + t;
      woff[tau] = 16384 + (U >> 2) * 8192 + (8 * g + q) * 128 + (((U & 3) ^ swz) << 5) + pp * 8;
    }
  }
  f32x4 acc[2][5];
#pragma unroll
  for (int m = 0; m < 2; ++m)
#pragma unroll
    for (int t = 0; t < 5; ++t) acc[m][t] = f32x4{0, 0, 0, 0};
  f32x4 accS[2] = {f32x4{0, 0, 0, 0}, f32x4{0, 0, 0, 0}};
  f32x4 accC[5];
#pragma unroll
  for (int t = 0; t < 5; ++t) accC[t] = f32x4{0, 0, 0, 0};
  const bf16x8 ones = {0x3F80, 0x3F80, 0x3F80, 0x3F80, 0x3F80, 0x3F80, 0x3F80, 0x3F80};

  stage(0, 0);
  stage(1, 1);
  int slot = 0;
  for (int ch = 0; ch < 10; ++ch) {
    if (ch < 9) WAITV(5); else WAITV(0);
    WAITL0();
    BAR();
    if (ch + 2 < 10) { int s2 = slot + 2; if (s2 >= 3) s2 -= 3; stage(ch + 2, s2); }
    LAS char* base = L + slot * 40960;
#pragma unroll
    for (int ks = 0; ks < 2; ++ks) {
      bf16x8 xf[2], wf[5];
#pragma unroll
      for (int m = 0; m < 2; ++m) xf[m] = *(LAS bf16x8*)(base + xoff[ks] + m * 2048);
#pragma unroll
      for (int t = 0; t < 5; ++t) wf[t] = cat(trread(base + woff[t] + ks * 4096), trread(base + woff[t] + ks * 4096 + 512));
#pragma unroll
      for (int t = 0; t < 5; ++t)
#pragma unroll
        for (int m = 0; m < 2; ++m) acc[m][t] = mfma(wf[t], xf[m], acc[m][t]);
      if (ISW1) {
#pragma unroll
        for (int m = 0; m < 2; ++m) accS[m] = mfma(wn == 0 ? ones : xf[m], xf[m], accS[m]);
        if (((2 * ch + ks) & 3) == wm) {
#pragma unroll
          for (int t = 0; t < 5; ++t) accC[t] = mfma(wf[t], ones, accC[t]);
        }
      }
    }
    if (++slot == 3) slot = 0;
  }
  if (!ISW1) {
    char* dst = p.ws + WS_QKV;
#pragma unroll
    for (int m = 0; m < 2; ++m) {
      size_t rt = rt128 * 8 + wm * 2 + m;
      char* fb = dst + (rt * 60 + 5 * c) * 1024 + lane * 16;
      int p0 = wn == 0 ? 0 : 3;
#pragma unroll
      for (int pr = 0; pr < 2; ++pr) {
        f32x4 a = acc[m][2 * pr], b = acc[m][2 * pr + 1];
        v4u o = {pk2(a[0], a[1]), pk2(a[2], a[3]), pk2(b[0], b[1]), pk2(b[2], b[3])};
        *(v4u*)(fb + (p0 + pr) * 1024) = o;
      }
      f32x4 a = acc[m][4];
      v2u o = {pk2(a[0], a[1]), pk2(a[2], a[3])};
      *(v2u*)(fb + 2 * 1024 + wn * 8) = o;
    }
  } else {
    __syncthreads();
    LAS float* rs = (LAS float*)L;
    LAS float* cs = (LAS float*)(L + 1024);
#pragma unroll
    for (int m = 0; m < 2; ++m) {
      if (wn == 0) { if (g == 0) rs[wm * 32 + m * 16 + r] = accS[m][0]; }
      else { if (g == (r >> 2)) { float v = (r & 3) == 0 ? accS[m][0] : (r & 3) == 1 ? accS[m][1] : (r & 3) == 2 ? accS[m][2] : accS[m][3]; rs[128 + wm * 32 + m * 16 + r] = v; } }
    }
    if (r == 0) {
#pragma unroll
      for (int tau = 0; tau < 5; ++tau) { int t = 5 * wn + (tau + wn) % 5; *(LAS f32x4*)(cs + wm * 160 + t * 16 + g * 4) = accC[tau]; }
    }
    __syncthreads();
    f32x4 csum[5];
#pragma unroll
    for (int tau = 0; tau < 5; ++tau) {
      int t = 5 * wn + (tau + wn) % 5;
      csum[tau] = *(LAS f32x4*)(cs + t * 16 + g * 4) + *(LAS f32x4*)(cs + 160 + t * 16 + g * 4) + *(LAS f32x4*)(cs + 320 + t * 16 + g * 4) + *(LAS f32x4*)(cs + 480 + t * 16 + g * 4);
    }
    const int delta = cc == 0 ? 1 : cc == 1 ? -1 : cc == 2 ? 2 : -2;
#pragma unroll
    for (int m = 0; m < 2; ++m) {
      float sum = rs[wm * 32 + m * 16 + r], sq = rs[128 + wm * 32 + m * 16 + r];
      float mean = sum * (1.0f / D);
      float var = fmaxf(sq * (1.0f / D) - mean * mean, 0.0f);
      float rstd = __builtin_amdgcn_rsqf(var + 1e-5f);
      int srow_ = rt128 * 128 + wm * 32 + m * 16 + r;
      int drow = srow_ + delta;
      bool wrap = (unsigned)drow >= (unsigned)S;
      drow &= (S - 1);
      char* fb = p.ws + WS_HS + ((size_t)(drow >> 4) * 20 + 5 * cc) * 1024 + (g * 16 + (drow & 15)) * 16;
      unsigned pkd[5][2];
#pragma unroll
      for (int tau = 0; tau < 5; ++tau) {
        float h[4];
#pragma unroll
        for (int e = 0; e < 4; ++e) h[e] = gelu_t(rstd * (acc[m][tau][e] - mean * csum[tau][e]));
        pkd[tau][0] = wrap ? 0u : pk2(h[0], h[1]);
        pkd[tau][1] = wrap ? 0u : pk2(h[2], h[3]);
      }
      int p0 = wn == 0 ? 0 : 3;
#pragma unroll
      for (int pr = 0; pr < 2; ++pr) { v4u o = {pkd[2 * pr][0], pkd[2 * pr][1], pkd[2 * pr + 1][0], pkd[2 * pr + 1][1]}; *(v4u*)(fb + (p0 + pr) * 1024) = o; }
      v2u o = {pkd[4][0], pkd[4][1]};
      *(v2u*)(fb + 2 * 1024 + wn * 8) = o;
    }
  }
}

DEV void retile_job(const Params& p, LAS char* Lw, int idx, int lane) {
  const u16* W; int pitch, KS, cb, ks; size_t dst;
  if (idx < 200) { W = p.w2; pitch = 1280; KS = 20; cb = idx / 20; ks = idx % 20; dst = WS_W2; }
  else if (idx < 400) { int i = idx - 200; W = p.wo; pitch = 1280; KS = 20; cb = i / 20; ks = i % 20; dst = WS_WO; }
  else if (idx < 940) { int i = idx - 400; W = p.w3; pitch = 3456; KS = 20; cb = i / 20; ks = i % 20; dst = WS_W3; }
  else { int i = idx - 940; W = p.w4; pitch = 1280; KS = 54; cb = i / 54; ks = i % 54; dst = WS_W4; }
  const int srow = lane >> 3, ss = lane & 7;
#pragma unroll
  for (int q = 0; q < 4; ++q) {
    int row = 8 * q + srow;
    int swz = (row >> 1) & 3;
    glds16((const char*)W + (size_t)(32 * ks + row) * pitch + cb * 128 + (((((ss >> 1) ^ swz) << 1) | (ss & 1)) << 4), Lw + q * 1024);
  }
  WAITV(0);
  const int g = lane >> 4, q = (lane >> 2) & 3, pp = lane & 3;
  const int row = 4 * g + q;
  const int swz = (row >> 1) & 3;
#pragma unroll
  for (int nt = 0; nt < 4; ++nt) {
    LAS char* a = Lw + row * 128 + ((nt ^ swz) << 5) + pp * 8;
    bf16x8 f = cat(trread(a), trread(a + 2048));
    *(bf16x8*)(p.ws + dst + ((size_t)(4 * cb + nt) * KS + ks) * 1024 + lane * 16) = f;
  }
}

__global__ __launch_bounds__(512, 2) void k1(Params p) {
  extern __shared__ __attribute__((aligned(16))) char smem[];
  LAS char* L = (LAS char*)smem;
  const int b = blockIdx.x, tid = threadIdx.x, lane = tid & 63;
  const int w = __builtin_amdgcn_readfirstlane(tid >> 6);
  const int xcd = b & 7, j = b >> 3, grp = xcd >> 1, half = xcd & 1;
  const int rt128 = 4 * grp + (j & 3), c = 8 * half + (j >> 2);
  int idx = w * 256 + b;
  if (idx < 1480) retile_job(p, L + 122880 + w * 4096, idx, lane);
  if (c < 12) s1_main<false>(p, L, rt128, c, w, lane);
  else s1_main<true>(p, L, rt128, c, w, lane);
}

template <int NKS>
DEV void gemm64x80(const char* Aft, const char* Bwt, LAS char* L, int rt64, int ct, int w, int lane, f32x4 (&acc)[5]) {
  const int wm = w & 3, wk = w >> 2;
  constexpr int NCH = (NKS + 3) / 4;
  constexpr int NSLOT = 6;
  constexpr int NA = (NKS + 1) / 2;
  static_assert(NKS % 2 == 0, "");
  auto stage = [&](int ch, int slot) {
#pragma unroll
    for (int i = 0; i < 3; ++i) {
      int f = w + 8 * i; if (f >= 20) f -= 4;
      int ksl = f / 5, tau = f % 5;
      int ks = 4 * ch + ksl; if (ks >= NKS) ks = NKS - 1;
      int t = (tau + (ct & 1)) % 5;
      glds16(Bwt + ((size_t)(5 * ct + t) * NKS + ks) * 1024 + lane * 16, L + slot * 20480 + f * 1024);
    }
  };
  bf16x8 af[NA];
  const char* ap = Aft + ((size_t)(rt64 * 4 + wm) * NKS + wk) * 1024 + lane * 16;
#pragma unroll
  for (int i = 0; i < NA; ++i) af[i] = *(const bf16x8*)(ap + i * 2048);
#pragma unroll
  for (int c = 0; c < NSLOT - 1; ++c) if (c < NCH) stage(c, c);
#pragma unroll
  for (int t = 0; t < 5; ++t) acc[t] = f32x4{0, 0, 0, 0};
  WAITV(0);
#pragma unroll
  for (int ch = 0; ch < NCH; ++ch) {
    if (ch >= NSLOT - 1) {
      constexpr int dummy = 0; (void)dummy;
      int rem = (NCH - 1 - ch) < (NSLOT - 2) ? (NCH - 1 - ch) : (NSLOT - 2);
      if (rem == 4) WAITV(12); else if (rem == 3) WAITV(9); else if (rem == 2) WAITV(6); else if (rem == 1) WAITV(3); else WAITV(0);
    }
    WAITL0();
    BAR();
    if (ch + NSLOT - 1 < NCH) stage(ch + NSLOT - 1, (ch + NSLOT - 1) % NSLOT);
    LAS char* base = L + (ch % NSLOT) * 20480 + lane * 16;
#pragma unroll
    for (int i = 0; i < 2; ++i) {
      int ai = 2 * ch + i;
      if (ai < NA) {
        bf16x8 bfr[5];
#pragma unroll
        for (int t = 0; t < 5; ++t) bfr[t] = *(LAS bf16x8*)(base + ((2 * i + wk) * 5 + t) * 1024);
#pragma unroll
        for (int t = 0; t < 5; ++t) acc[t] = mfma(bfr[t], af[ai], acc[t]);
      }
    }
  }
  __syncthreads();
  LAS f32x4* red = (LAS f32x4*)L;
  if (wk == 0) { red[(wm * 5 + 2) * 64 + lane] = acc[2]; red[(wm * 5 + 3) * 64 + lane] = acc[3]; }
  else { red[(wm * 5 + 0) * 64 + lane] = acc[0]; red[(wm * 5 + 1) * 64 + lane] = acc[1]; red[(wm * 5 + 4) * 64 + lane] = acc[4]; }
  __syncthreads();
  if (wk == 0) { acc[0] += red[(wm * 5 + 0) * 64 + lane]; acc[1] += red[(wm * 5 + 1) * 64 + lane]; acc[2] = acc[4] + red[(wm * 5 + 4) * 64 + lane]; }
  else { acc[0] = acc[2] + red[(wm * 5 + 2) * 64 + lane]; acc[1] = acc[3] + red[(wm * 5 + 3) * 64 + lane]; acc[2] = f32x4{0, 0, 0, 0}; }
  __syncthreads();
}
DEV int ntile_of(int ct, int wk, int i) { int tau = i == 2 ? 4 : 2 * wk + i; return 5 * ct + (tau + (ct & 1)) % 5; }

DEV float xsum4(float v) { v += __shfl_xor(v, 16); v += __shfl_xor(v, 32); return v; }
DEV float xmax4(float v) { v = fmaxf(v, __shfl_xor(v, 16)); v = fmaxf(v, __shfl_xor(v, 32)); return v; }

DEV void attention(const Params& p, LAS char* L, int rb, int hg, int w, int lane) {
  const char* qkv = p.ws + WS_QKV;
#pragma unroll
  for (int i = 0; i < 5; ++i) {
    int f = w * 5 + i;
    int rt, pb;
    if (w < 2) { rt = 2 * rb + w; pb = 0; } else if (w < 5) { rt = 2 * rb - 1 + (w - 2); pb = 20; } else { rt = 2 * rb - 1 + (w - 5); pb = 40; }
    if (rt < 0) rt = 0;
    glds16(qkv + ((size_t)rt * 60 + pb + 5 * hg + i) * 1024 + lane * 16, L + f * 1024);
  }
  WAITV(0);
  __syncthreads();
  const int hh = w, r = lane & 15, g = lane >> 4;
  auto qaddr = [&](int Q, int row) { return (Q >> 3) * 1024 + ((Q & 3) * 16 + row) * 16 + ((Q >> 2) & 1) * 8; };
  const int a0 = qaddr(5 * hh + g, r), a1 = qaddr(5 * hh + 4, r);
  bf16x8 kf[3];
#pragma unroll
  for (int t = 0; t < 3; ++t) kf[t] = cat(*(LAS bf16x4*)(L + (10 + 5 * t) * 1024 + a0), *(LAS bf16x4*)(L + (10 + 5 * t) * 1024 + a1));
  const int q4 = (lane >> 2) & 3, pp = lane & 3;
  const int v0 = qaddr(5 * hh + pp, 4 * g + q4), v1 = qaddr(5 * hh + 4, 4 * g + q4);
  bf16x4 vt0[3], vt1[3];
#pragma unroll
  for (int t = 0; t < 3; ++t) { vt0[t] = trread(L + (25 + 5 * t) * 1024 + v0); vt1[t] = trread(L + (25 + 5 * t) * 1024 + v1); }
  LAS char* Lo = L + 40960;
#pragma unroll
  for (int qb = 0; qb < 2; ++qb) {
    bf16x4 qlo = *(LAS bf16x4*)(L + (5 * qb) * 1024 + a0), qhi = *(LAS bf16x4*)(L + (5 * qb) * 1024 + a1);
    if (g != 0) qhi = bf16x4{0, 0, 0, 0};
    bf16x8 qf = cat(qlo, qhi);
    f32x4 z = {0, 0, 0, 0};
    f32x4 sp = mfma(kf[qb], qf, z), sc = mfma(kf[qb + 1], qf, z);
    const bool prev_ok = (rb | qb) != 0;
    const float NEG = -__builtin_inff();
    float mx = NEG;
#pragma unroll
    for (int e = 0; e < 4; ++e) {
      int dp = r + 16 - 4 * g - e;
      int dc = r - 4 * g - e;
      bool vp = prev_ok && ((dp & 1) == 0) && dp <= 14;
      bool vc = dc >= 0 && ((dc & 1) == 0) && dc <= 14;
      sp[e] = vp ? sp[e] : NEG; sc[e] = vc ? sc[e] : NEG;
      mx = fmaxf(mx, fmaxf(sp[e], sc[e]));
    }
    mx = xmax4(mx);
    const float cexp = 0.22360679774997896f * 1.4426950408889634f;
    float sum = 0;
#pragma unroll
    for (int e = 0; e < 4; ++e) {
      sp[e] = __builtin_amdgcn_exp2f((sp[e] - mx) * cexp); sc[e] = __builtin_amdgcn_exp2f((sc[e] - mx) * cexp);
      sum += sp[e] + sc[e];
    }
    sum = xsum4(sum);
    v4u pu = {pk2(sp[0], sp[1]), pk2(sp[2], sp[3]), pk2(sc[0], sc[1]), pk2(sc[2], sc[3])};
    bf16x8 pf = __builtin_bit_cast(bf16x8, pu);
    f32x4 o0 = mfma(cat(vt0[qb], vt0[qb + 1]), pf, z), o1 = mfma(cat(vt1[qb], vt1[qb + 1]), pf, z);
    float inv = __builtin_amdgcn_rcpf(sum);
    v2u w0 = {pk2(o0[0] * inv, o0[1] * inv), pk2(o0[2] * inv, o0[3] * inv)};
    *(LAS v2u*)(Lo + qb * 5120 + a0) = w0;
    if (g == 0) { v2u w1 = {pk2(o1[0] * inv, o1[1] * inv), pk2(o1[2] * inv, o1[3] * inv)}; *(LAS v2u*)(Lo + qb * 5120 + a1) = w1; }
  }
  __syncthreads();
#pragma unroll
  for (int i = 0; i < 2; ++i) {
    int f = w + 8 * i;
    if (f < 10) {
      v4u v = *(LAS v4u*)(Lo + f * 1024 + lane * 16);
      *(v4u*)(p.ws + WS_O + ((size_t)(2 * rb + f / 5) * 20 + 5 * hg + f % 5) * 1024 + lane * 16) = v;
    }
  }
}

__global__ __launch_bounds__(512, 2) void k2(Params p) {
  extern __shared__ __attribute__((aligned(16))) char smem[];
  LAS char* L = (LAS char*)smem;
  const int b = blockIdx.x, tid = threadIdx.x, lane = tid & 63;
  const int w = __builtin_amdgcn_readfirstlane(tid >> 6);
  const int xcd = b & 7, j = b >> 3, grp = xcd >> 1, half = xcd & 1;
  const int rt64 = 8 * grp + (j & 7), ct = 4 * half + (j >> 3);
  const int wm = w & 3, wk = w >> 2, r = lane & 15, g = lane >> 4;
  const int row = rt64 * 64 + wm * 16 + r;
  v2u xr[3], gwr[3];
#pragma unroll
  for (int i = 0; i < 3; ++i) { int nt = ntile_of(ct, wk, i); xr[i] = *(const v2u*)(p.x + (size_t)row * D + nt * 16 + g * 4); gwr[i] = *(const v2u*)(p.gw + nt * 16 + g * 4); }
  f32x4 acc[5];
  gemm64x80<20>(p.ws + WS_HS, p.ws + WS_W2, L, rt64, ct, w, lane, acc);
  {
    const int nv = wk == 0 ? 3 : 2;
    unsigned pk[3][2]; float v[3][4]; float sum = 0, gs = 0;
#pragma unroll
    for (int i = 0; i < 3; ++i) {
      float x0 = bflo(xr[i][0]), x1 = bfhi(xr[i][0]), x2 = bflo(xr[i][1]), x3 = bfhi(xr[i][1]);
      pk[i][0] = pk2(x0 + acc[i][0], x1 + acc[i][1]); pk[i][1] = pk2(x2 + acc[i][2], x3 + acc[i][3]);
      v[i][0] = bflo(pk[i][0]); v[i][1] = bfhi(pk[i][0]); v[i][2] = bflo(pk[i][1]); v[i][3] = bfhi(pk[i][1]);
      if (i < nv) {
        sum += (v[i][0] + v[i][1]) + (v[i][2] + v[i][3]);
        gs += x0 * bflo(gwr[i][0]) + x1 * bfhi(gwr[i][0]) + x2 * bflo(gwr[i][1]) + x3 * bfhi(gwr[i][1]);
      }
    }
    sum = xsum4(sum); gs = xsum4(gs);
    float lm = sum / (float)(16 * nv), m2 = 0;
#pragma unroll
    for (int i = 0; i < 3; ++i) if (i < nv) {
#pragma unroll
      for (int e = 0; e < 4; ++e) { float d = v[i][e] - lm; m2 += d * d; }
    }
    m2 = xsum4(m2);
    if (g == 0) *(f32x4*)(p.ws + WS_ST + ((size_t)row * 16 + ct * 2 + wk) * 16) = f32x4{sum, m2, gs, 0.f};
    char* fb = p.ws + WS_X1 + (size_t)(rt64 * 4 + wm) * 20 * 1024 + lane * 16;
    int nt0 = ntile_of(ct, wk, 0);
    *(v4u*)(fb + (nt0 >> 1) * 1024) = v4u{pk[0][0], pk[0][1], pk[1][0], pk[1][1]};
    if (wk == 0) { int nt2 = ntile_of(ct, 0, 2); *(v2u*)(fb + (nt2 >> 1) * 1024 + (nt2 & 1) * 8) = v2u{pk[2][0], pk[2][1]}; }
  }
  const int idx = half * 32 + j;
  attention(p, L, 16 * grp + (idx & 15), idx >> 4, w, lane);
}

__global__ __launch_bounds__(512, 2) void k3(Params p) {
  extern __shared__ __attribute__((aligned(16))) char smem[];
  LAS char* L = (LAS char*)smem;
  const int b = blockIdx.x, tid = threadIdx.x, lane = tid & 63;
  const int w = __builtin_amdgcn_readfirstlane(tid >> 6);
  const int xcd = b & 7, j = b >> 3, grp = xcd >> 1, half = xcd & 1;
  const int r = lane & 15, g = lane >> 4;
  {
    const int rt128 = 4 * grp + (j & 3), cg = 8 * half + (j >> 2);
    const int st = 27 * (cg >> 2) + ((cg & 3) == 3 ? 20 : 7 * (cg & 3));
    const int rt = rt128 * 8 + w;
    const int row = rt * 16 + r;
    for (int f = w; f < 140; f += 8) {
      int ks = f / 7, tau = f % 7; int t = (tau + (st & 1)) % 7;
      glds16(p.ws + WS_W3 + ((size_t)(st + t) * 20 + ks) * 1024 + lane * 16, L + f * 1024);
    }
    bf16x8 af[20];
    const char* ap = p.ws + WS_X1 + (size_t)rt * 20 * 1024 + lane * 16;
#pragma unroll
    for (int i = 0; i < 20; ++i) af[i] = *(const bf16x8*)(ap + i * 1024);
    f32x4 stp[4];
#pragma unroll
    for (int i = 0; i < 4; ++i) stp[i] = *(const f32x4*)(p.ws + WS_ST + ((size_t)row * 16 + g * 4 + i) * 16);
    WAITV(0);
    __syncthreads();
    f32x4 acc[7], accC[7];
#pragma unroll
    for (int t = 0; t < 7; ++t) { acc[t] = f32x4{0, 0, 0, 0}; accC[t] = f32x4{0, 0, 0, 0}; }
    const bf16x8 ones = {0x3F80, 0x3F80, 0x3F80, 0x3F80, 0x3F80, 0x3F80, 0x3F80, 0x3F80};
#pragma unroll
    for (int ks = 0; ks < 20; ++ks) {
      bf16x8 bfr[7];
#pragma unroll
      for (int t = 0; t < 7; ++t) bfr[t] = *(LAS bf16x8*)(L + (ks * 7 + t) * 1024 + lane * 16);
#pragma unroll
      for (int t = 0; t < 7; ++t) acc[t] = mfma(bfr[t], af[ks], acc[t]);
      if ((ks & 7) == w) {
#pragma unroll
        for (int t = 0; t < 7; ++t) accC[t] = mfma(bfr[t], ones, accC[t]);
      }
    }
    LAS float* cs = (LAS float*)(L + 143360);
    if (r == 0) {
#pragma unroll
      for (int t = 0; t < 7; ++t) *(LAS f32x4*)(cs + w * 112 + t * 16 + g * 4) = accC[t];
    }
    __syncthreads();
    float sum = 0;
#pragma unroll
    for (int i = 0; i < 4; ++i) sum += stp[i][0];
    sum = xsum4(sum);
    float mean = sum * (1.0f / D), m2 = 0;
#pragma unroll
    for (int i = 0; i < 4; ++i) { float n = (i & 1) ? 32.f : 48.f; float d = stp[i][0] / n - mean; m2 += stp[i][1] + n * d * d; }
    m2 = xsum4(m2);
    float rstd = __builtin_amdgcn_rsqf(m2 * (1.0f / D) + 1e-5f);
    unsigned pk[7][2];
#pragma unroll
    for (int t = 0; t < 7; ++t) {
      f32x4 c = {0, 0, 0, 0};
#pragma unroll
      for (int ww = 0; ww < 8; ++ww) c += *(LAS f32x4*)(cs + ww * 112 + t * 16 + g * 4);
      float h[4];
#pragma unroll
      for (int e = 0; e < 4; ++e) h[e] = gelu_t(rstd * (acc[t][e] - mean * c[e]));
      pk[t][0] = pk2(h[0], h[1]); pk[t][1] = pk2(h[2], h[3]);
    }
    char* fb = p.ws + WS_U + (size_t)rt * 54 * 1024 + lane * 16;
    const int pb = (st + (st & 1)) >> 1;
#pragma unroll
    for (int pr = 0; pr < 3; ++pr) *(v4u*)(fb + (pb + pr) * 1024) = v4u{pk[2 * pr][0], pk[2 * pr][1], pk[2 * pr + 1][0], pk[2 * pr + 1][1]};
    int nts = st + (6 + (st & 1)) % 7;
    *(v2u*)(fb + (nts >> 1) * 1024 + (nts & 1) * 8) = v2u{pk[6][0], pk[6][1]};
    __syncthreads();
  }
  {
    const int rt64 = 8 * grp + (j & 7), ct = 4 * half + (j >> 3);
    f32x4 acc[5];
    gemm64x80<20>(p.ws + WS_O, p.ws + WS_WO, L, rt64, ct, w, lane, acc);
#pragma unroll
    for (int i = 0; i < 3; ++i) *(f32x4*)(p.ws + WS_A + ((size_t)(b * 8 + w) * 3 + i) * 1024 + lane * 16) = acc[i];
  }
}

__global__ __launch_bounds__(512, 2) void k4(Params p) {
  extern __shared__ __attribute__((aligned(16))) char smem[];
  LAS char* L = (LAS char*)smem;
  const int b = blockIdx.x, tid = threadIdx.x, lane = tid & 63;
  const int w = __builtin_amdgcn_readfirstlane(tid >> 6);
  const int xcd = b & 7, j = b >> 3, grp = xcd >> 1, half = xcd & 1;
  const int rt64 = 8 * grp + (j & 7), ct = 4 * half + (j >> 3);
  const int wm = w & 3, wk = w >> 2, r = lane & 15, g = lane >> 4;
  const int row = rt64 * 64 + wm * 16 + r;
  f32x4 av[3]; v2u x1r[3];
#pragma unroll
  for (int i = 0; i < 3; ++i) {
    av[i] = *(const f32x4*)(p.ws + WS_A + ((size_t)(b * 8 + w) * 3 + i) * 1024 + lane * 16);
    int nt = ntile_of(ct, wk, i);
    x1r[i] = *(const v2u*)(p.ws + WS_X1 + ((size_t)(rt64 * 4 + wm) * 20 + (nt >> 1)) * 1024 + lane * 16 + (nt & 1) * 8);
  }
  float gs = 0;
#pragma unroll
  for (int i = 0; i < 4; ++i) gs += *(const float*)(p.ws + WS_ST + ((size_t)row * 16 + g * 4 + i) * 16 + 8);
  f32x4 acc[5];
  gemm64x80<54>(p.ws + WS_U, p.ws + WS_W4, L, rt64, ct, w, lane, acc);
  gs = xsum4(gs);
  const float gate = 1.0f / (1.0f + __expf(-gs));
  const int nv = wk == 0 ? 3 : 2;
#pragma unroll
  for (int i = 0; i < 3; ++i) if (i < nv) {
    float b0 = bflo(x1r[i][0]) + acc[i][0], b1 = bfhi(x1r[i][0]) + acc[i][1], b2 = bflo(x1r[i][1]) + acc[i][2], b3 = bfhi(x1r[i][1]) + acc[i][3];
    float o0 = gate * av[i][0] + (1.f - gate) * b0, o1 = gate * av[i][1] + (1.f - gate) * b1, o2 = gate * av[i][2] + (1.f - gate) * b2, o3 = gate * av[i][3] + (1.f - gate) * b3;
    int tl = ntile_of(ct, wk, i) - 5 * ct;
    *(LAS v2u*)(L + (wm * 16 + r) * 176 + tl * 32 + g * 8) = v2u{pk2(o0, o1), pk2(o2, o3)};
  }
  __syncthreads();
  for (int i = tid; i < 640; i += 512) {
    int rr = i / 10, cq = i % 10;
    v4u v = *(LAS v4u*)(L + rr * 176 + cq * 16);
    *(v4u*)((char*)p.out + (size_t)(rt64 * 64 + rr) * 1280 + ct * 160 + cq * 16) = v;
  }
}

extern "C" void kernel_launch(void* const* d_in, const int* in_sizes, int n_in, void* d_out, int out_size, void* d_ws, size_t ws_size, hipStream_t stream) {
  Params p;
  p.x = (const u16*)d_in[0]; p.wqkv = (const u16*)d_in[1]; p.wo = (const u16*)d_in[2];
  p.w1 = (const u16*)d_in[4]; p.w2 = (const u16*)d_in[5]; p.w3 = (const u16*)d_in[6]; p.w4 = (const u16*)d_in[7]; p.gw = (const u16*)d_in[8];
  p.out = (u16*)d_out; p.ws = (char*)d_ws;
  const int l1 = 155648, l2 = 122880, l3 = 147456, l4 = 122880;
  hipFuncSetAttribute((const void*)k1, hipFuncAttributeMaxDynamicSharedMemorySize, l1);
  hipFuncSetAttribute((const void*)k2, hipFuncAttributeMaxDynamicSharedMemorySize, l2);
  hipFuncSetAttribute((const void*)k3, hipFuncAttributeMaxDynamicSharedMemorySize, l3);
  hipFuncSetAttribute((const void*)k4, hipFuncAttributeMaxDynamicSharedMemorySize, l4);
  k1<<<256, 512, l1, stream>>>(p);
  k2<<<256, 512, l2, stream>>>(p);
  k3<<<256, 512, l3, stream>>>(p);
  k4<<<256, 512, l4, stream>>>(p);
}
```

```cpp
#include <hip/hip_runtime.h>
#include <stdint.h>
#include <type_traits>

typedef unsigned short u16;
typedef unsigned long long u64;
typedef short bf16x8 __attribute__((ext_vector_type(8)));
typedef short bf16x4 __attribute__((ext_vector_type(4)));
typedef float f32x4 __attribute__((ext_vector_type(4)));
typedef float f32x2 __attribute__((ext_vector_type(2)));
typedef __bf16 bfv2 __attribute__((ext_vector_type(2)));
typedef unsigned v4u __attribute__((ext_vector_type(4)));
typedef unsigned v2u __attribute__((ext_vector_type(2)));
#define LAS __attribute__((address_space(3)))
#define GAS __attribute__((address_space(1)))
#define DEV __device__ __forceinline__
#define RLX_AGENT __ATOMIC_RELAXED, __HIP_MEMORY_SCOPE_AGENT

constexpr int S = 2048, D = 640, F = 1728;
constexpr unsigned KB = 1024;
constexpr unsigned WS_W2 = 0;
constexpr unsigned WS_WO = WS_W2 + 800 * KB;
constexpr unsigned WS_W3 = WS_WO + 800 * KB;
constexpr unsigned WS_W4 = WS_W3 + 2240 * KB;
constexpr unsigned WS_QKV = WS_W4 + 2160 * KB;
constexpr unsigned WS_HS = WS_QKV + 7680 * KB;
constexpr unsigned WS_O = WS_HS + 2560 * KB;
constexpr unsigned WS_X1 = WS_O + 2560 * KB;
constexpr unsigned WS_U = WS_X1 + 2560 * KB;
constexpr unsigned WS_ST = WS_U + 6912 * KB;
constexpr unsigned WS_RS = WS_ST + 256 * KB;
constexpr unsigned WS_CS1 = WS_RS + 32 * KB;
constexpr unsigned WS_CS3 = WS_CS1 + 52 * KB;
constexpr unsigned WS_F0 = WS_CS3 + 136 * KB;
constexpr unsigned WS_EP = WS_F0 + 4 * KB;
constexpr unsigned WS_F1 = WS_EP + 4 * KB;
constexpr unsigned WS_F2 = WS_F1 + 32 * KB;
constexpr unsigned WS_FA = WS_F2 + 32 * KB;
constexpr unsigned WS_F3 = WS_FA + 32 * KB;
constexpr unsigned WS_END = WS_F3 + 32 * KB;
constexpr unsigned OOB = 0x40000000u;
constexpr unsigned L_MISC = 140 * KB;

DEV f32x4 mfma(bf16x8 a, bf16x8 b, f32x4 c) { return __builtin_amdgcn_mfma_f32_16x16x32_bf16(a, b, c, 0, 0, 0); }
DEV unsigned pk2(float a, float b) { f32x2 v = {a, b}; bfv2 r = __builtin_convertvector(v, bfv2); return __builtin_bit_cast(unsigned, r); }
DEV float bflo(unsigned d) { return __uint_as_float(d << 16); }
DEV float bfhi(unsigned d) { return __uint_as_float(d & 0xffff0000u); }
DEV float dot2(unsigned a, unsigned b, float c) { return __builtin_amdgcn_fdot2_f32_bf16(__builtin_bit_cast(bfv2, a), __builtin_bit_cast(bfv2, b), c, false); }
DEV float gelu_t(float x) {
  const float c1 = -2.0f * 0.7978845608028654f * 1.4426950408889634f;
  const float c2 = c1 * 0.044715f;
  float t = x * __builtin_fmaf(x * x, c2, c1);
  float e = __builtin_amdgcn_exp2f(t);
  return x * __builtin_amdgcn_rcpf(1.0f + e);
}
template <int IMM> DEV void glds(const void* sbase, unsigned voff, unsigned lds) {
  asm volatile("s_mov_b32 m0, %2\n\ts_nop 0\n\tglobal_load_lds_dwordx4 %0, %1 offset:%3 sc1" ::"v"(voff), "s"(sbase), "s"(lds), "n"(IMM) : "memory");
}
template <int IMM> DEV void glds_same_m0(const void* sbase, unsigned voff) {
  asm volatile("global_load_lds_dwordx4 %0, %1 offset:%2 sc1" ::"v"(voff), "s"(sbase), "n"(IMM) : "memory");
}
template <int IMM, typename T> DEV void gload16(T& d, const void* sbase, unsigned voff) {
  asm volatile("global_load_dwordx4 %0, %1, %2 offset:%3 sc1" : "=v"(d) : "v"(voff), "s"(sbase), "n"(IMM) : "memory");
}
template <int IMM, typename T> DEV void gload4(T& d, const void* sbase, unsigned voff) {
  asm volatile("global_load_dword %0, %1, %2 offset:%3 sc1" : "=v"(d) : "v"(voff), "s"(sbase), "n"(IMM) : "memory");
}
template <typename T> DEV void gload8(T& d, const void* sbase, unsigned voff) {
  asm volatile("global_load_dwordx2 %0, %1, %2 sc1" : "=v"(d) : "v"(voff), "s"(sbase) : "memory");
}
DEV bf16x4 trread(LAS char* p) { return __builtin_amdgcn_ds_read_tr16_b64_v4i16((LAS bf16x4*)p); }
DEV bf16x8 cat(bf16x4 a, bf16x4 b) { return __builtin_shufflevector(a, b, 0, 1, 2, 3, 4, 5, 6, 7); }
#define WAITV(n) asm volatile("s_waitcnt vmcnt(%0)" ::"n"(n) : "memory")
#define WAITV_R(n, r) asm volatile("s_waitcnt vmcnt(%1)" : "+v"(r) : "n"(n) : "memory")
#define PIN(r) asm volatile("" : "+v"(r))
#define BAR() asm volatile("s_waitcnt lgkmcnt(0)\n\ts_barrier" ::: "memory")
#define RAWBAR() asm volatile("s_barrier" ::: "memory")
#define SB() __builtin_amdgcn_sched_barrier(0)
#define AMFMA(acc, a, b) asm volatile("v_mfma_f32_16x16x32_bf16 %0, %1, %2, %0" : "+v"(acc) : "v"(a), "v"(b))
#define ADSR(dst, addr, off) asm volatile("ds_read_b128 %0, %1 offset:%2" : "=v"(dst) : "v"(addr), "n"(off))
#define WAITL(n) asm volatile("s_waitcnt lgkmcnt(%0)" ::"n"(n))

#define STAMP(i) do {} while (0)

struct Params {
  const u16 *x, *wqkv, *wo, *w1, *w2, *w3, *w4, *gw;
  u16* out;
  char* ws;
};

template <int N, typename Fn> DEV void sfor_impl(Fn&& f) { if constexpr (N > 0) { sfor_impl<N - 1>(f); f(std::integral_constant<int, N - 1>{}); } }
template <int N, typename Fn> DEV void sfor(Fn&& f) { sfor_impl<N>(f); }

DEV float xsum4(float v) {
  unsigned u = __float_as_uint(v);
  auto a = __builtin_amdgcn_permlane16_swap(u, u, false, false);
  v = __uint_as_float(a[0]) + __uint_as_float(a[1]);
  u = __float_as_uint(v);
  auto b = __builtin_amdgcn_permlane32_swap(u, u, false, false);
  return __uint_as_float(b[0]) + __uint_as_float(b[1]);
}
DEV float xmax4(float v) {
  unsigned u = __float_as_uint(v);
  auto a = __builtin_amdgcn_permlane16_swap(u, u, false, false);
  v = fmaxf(__uint_as_float(a[0]), __uint_as_float(a[1]));
  u = __float_as_uint(v);
  auto b = __builtin_amdgcn_permlane32_swap(u, u, false, false);
  return fmaxf(__uint_as_float(b[0]), __uint_as_float(b[1]));
}
template <int CTRL> DEV float dpp_add(float v) { return v + __uint_as_float(__builtin_amdgcn_update_dpp(0, __float_as_uint(v), CTRL, 0xf, 0xf, true)); }
DEV float wavesum(float v) { v = dpp_add<0xB1>(v); v = dpp_add<0x4E>(v); v = dpp_add<0x141>(v); v = dpp_add<0x140>(v); return xsum4(v); }

DEV void poll_flags(const char* ws, unsigned off, u64 tag) {
  GAS u64* f = (GAS u64*)(ws + off);
#pragma unroll 1
  for (unsigned spins = 0; spins < (1u << 16); ++spins) {
    u64 v = __hip_atomic_load(f, RLX_AGENT);
    if (__all(v == tag)) break;
    __builtin_amdgcn_s_sleep(1);
  }
}
DEV void set_flag(char* ws, unsigned off, u64 tag) { __hip_atomic_store((GAS u64*)(ws + off), tag, RLX_AGENT); }

DEV int ntile_of(int ct, int wk, int i) { int t = (i == 2 ? 4 : 2 * wk + i) + (ct & 1); if (t >= 5) t -= 5; return 5 * ct + t; }

DEV void reduce_wk(LAS char* redb, int wm, int wk, int lane, f32x4 (&acc)[5]) {
  LAS f32x4* red = (LAS f32x4*)redb + wm * 5 * 64 + lane;
  if (wk == 0) { red[2 * 64] = acc[2]; red[3 * 64] = acc[3]; }
  else { red[0] = acc[0]; red[64] = acc[1]; red[4 * 64] = acc[4]; }
  BAR();
  if (wk == 0) { acc[0] += red[0]; acc[1] += red[64]; acc[2] = acc[4] + red[4 * 64]; }
  else { acc[0] = acc[2] + red[2 * 64]; acc[1] = acc[3] + red[3 * 64]; acc[2] = f32x4{0, 0, 0, 0}; }
}
#define MFMA_FENCE5(a) asm volatile("s_nop 7\n\ts_nop 1" : "+v"(a[0]), "+v"(a[1]), "+v"(a[2]), "+v"(a[3]), "+v"(a[4]))

template <int FRAG, int SLOT> DEV void bload(const char* sbw, unsigned l16, unsigned lw) { glds<0>(sbw + (size_t)FRAG * 1024, l16, lw + SLOT * 1024u); }

__global__ __launch_bounds__(512, 2) void mega(Params p) {
  extern __shared__ __attribute__((aligned(16))) char smem[];
  LAS char* L = (LAS char*)smem;
  const int b = blockIdx.x, tid = threadIdx.x, lane = tid & 63;
  const int w = __builtin_amdgcn_readfirstlane(tid >> 6);
  const int xcd = b & 7, j = b >> 3, grp = xcd >> 1, half = xcd & 1;
  const int rt128 = 4 * grp + (j & 3), c = 8 * half + (j >> 2);
  const int rt64 = 8 * grp + (j & 7), ct = 4 * half + (j >> 3);
  const int aidx = half * 32 + j, rb = 16 * grp + (aidx & 15), hg = aidx >> 4;
  const int wm = w & 3, wk = w >> 2, r = lane & 15, g = lane >> 4;
  const unsigned l16 = lane * 16, lw = w * 1024;
  const __amdgpu_buffer_rsrc_t rs = __builtin_amdgcn_make_buffer_rsrc((void*)p.ws, 0, WS_END, 0x00020000);
  const bf16x8 ones = {0x3F80, 0x3F80, 0x3F80, 0x3F80, 0x3F80, 0x3F80, 0x3F80, 0x3F80};
  u64 tag;
  STAMP(0);
  const bool isw1 = c >= 12;
  const int cc = isw1 ? c - 12 : c;
  f32x4 acc1[2][5];
  {
    const int wn = wk;
    const int pitch = isw1 ? 1280 : 3840;
    const char* sB = (const char*)(isw1 ? p.w1 : p.wqkv) + ((320 * cc) & ~127);
    const char* sA = (const char*)p.x + (size_t)rt128 * 128 * 1280;
    const int U0 = ((320 * cc) & 127) >> 5;
    const int srow = lane >> 3, ss = lane & 7;
    unsigned vo[5];
    {
      int r0 = 8 * w + srow;
      vo[0] = r0 * 1280 + ((ss ^ ((r0 >> 1) & 7)) << 4);
      vo[1] = vo[0] + 64 * 1280;
      int swz = ((r0 >> 1) & 1) | (((r0 >> 3) & 1) << 1);
      vo[2] = r0 * pitch + (((((ss >> 1) ^ swz) << 1) | (ss & 1)) << 4);
      vo[3] = vo[2] + 128; vo[4] = vo[2] + 256;
    }
    unsigned strA = 128, strB = 64 * pitch;
    unsigned mslot = 2 * 40960 + lw;
    auto issue = [&](auto K) {
      constexpr int k = K;
      glds<0>(k < 2 ? sA : sB, vo[k], mslot + k * 8192);
      vo[k] += (k < 2 ? strA : strB);
      if constexpr (k == 4) { mslot += 40960; if (mslot >= 163840) mslot -= 163840; }
    };
    v2u ep;
    asm volatile("global_load_dwordx2 %0, %1, %2 sc1" : "=v"(ep) : "v"(0u), "s"(p.ws + WS_EP) : "memory");
    sfor<5>(issue);
    {
      const u16* W; int rp = 1280, KS = 20, idx = b, typ = 0; bool valid = b < 200; unsigned dst = WS_W2;
      if (w == 0) { W = p.w2; }
      else if (w == 1) { W = p.wo; dst = WS_WO; }
      else if (w == 2) { W = p.w1; typ = 2; }
      else if (w < 5 || (w == 5 && b < 28)) { W = p.w3; rp = 3456; idx = (w - 3) * 256 + b; typ = 1; valid = true; dst = WS_W3; }
      else { W = p.w4; KS = 54; idx = w == 5 ? 512 + b - 28 : (w - 6) * 256 + b; valid = idx < 540; dst = WS_W4; }
      if (!valid) idx = 0;
      const int cb = KS == 20 ? idx / 20 : idx / 54, ks = idx - cb * KS;
      const int swz = (srow >> 1) & 3;
      const unsigned vr = srow * rp + (((((ss >> 1) ^ swz) << 1) | (ss & 1)) << 4);
      const char* sb = (const char*)W + (size_t)32 * ks * rp + cb * 128;
#pragma unroll
      for (int q = 0; q < 4; ++q) glds<0>(sb + (size_t)8 * q * rp, vr, 40960 + w * 4096 + q * 1024);
      const int srw = 512 * grp + aidx * 8 + w;
      v4u xa, xb, ga, gb;
      gload16<0>(xa, (const char*)p.x + (size_t)srw * 1280, l16);
      gload16<1024>(xb, (const char*)p.x + (size_t)srw * 1280, (unsigned)(r * 16));
      gload16<0>(ga, p.gw, l16);
      gload16<1024>(gb, p.gw, (unsigned)(r * 16));
      sfor<5>(issue);
      sfor<5>(issue);
      unsigned dv;
      {
        int li = lane & 7; int nt = 4 * cb + (li & 3);
        if (typ == 0) {
          int c5 = (nt * 205) >> 10, t = nt - 5 * c5, tau = t - (c5 & 1); if (tau < 0) tau += 5;
          dv = dst + ((c5 * KS + ks) * 5 + tau) * 1024; if (li >= 4) dv = OOB;
        } else {
          int a = (nt * 19) >> 9, rem = nt - 27 * a;
          int sub = rem < 7 ? 0 : rem < 14 ? 1 : rem < 21 ? 2 : 3;
          bool dup = li >= 4;
          if (dup) sub = 3;
          int so = sub == 3 ? 20 : 7 * sub; int t = rem - so; int par = (a + so) & 1;
          int tau = t - par; if (tau < 0) tau += 7;
          dv = dst + (((a * 4 + sub) * 20 + ks) * 7 + tau) * 1024;
          if (dup && rem != 20) dv = OOB;
        }
        if (!valid || typ == 2) dv = OOB;
      }
      STAMP(30);
      WAITV(10);
      STAMP(31);
      PIN(ep); PIN(xa); PIN(xb); PIN(ga); PIN(gb);
      const int q = (lane >> 2) & 3, pp = lane & 3;
      const int row = 4 * g + q, swz2 = (row >> 1) & 3;
      f32x4 csel = {0, 0, 0, 0};
      unsigned dupd = OOB; v4u dupv = {0, 0, 0, 0};
#pragma unroll
      for (int nt = 0; nt < 4; ++nt) {
        LAS char* a = L + 40960 + w * 4096 + row * 128 + ((nt ^ swz2) << 5) + pp * 8;
        bf16x8 f = cat(trread(a), trread(a + 2048));
        __builtin_amdgcn_raw_buffer_store_b128(__builtin_bit_cast(v4u, f), rs, l16 + __builtin_amdgcn_readlane(dv, nt), 0, 16);
        unsigned d2 = __builtin_amdgcn_readlane(dv, 4 + nt);
        if (d2 != OOB) { dupd = d2; dupv = __builtin_bit_cast(v4u, f); }
        f32x4 cs = mfma(f, ones, f32x4{0, 0, 0, 0});
        if (r == nt) csel = cs;
      }
      __builtin_amdgcn_raw_buffer_store_b128(dupv, rs, l16 + dupd, 0, 16);
      {
        unsigned cd = typ == 2 ? WS_CS1 + (ks * 640 + 64 * cb) * 4 : WS_CS3 + (ks * 1728 + 64 * cb) * 4;
        if (!valid || typ == 0) cd = OOB;
        __builtin_amdgcn_raw_buffer_store_b128(__builtin_bit_cast(v4u, csel), rs, r < 4 ? (16 * r + 4 * g) * 4 + cd : OOB, 0, 16);
      }
      if (lane >= 16) xb = v4u{0, 0, 0, 0};
      float sm = 0, sq = 0, gt = 0;
#pragma unroll
      for (int i = 0; i < 4; ++i) {
        sm = dot2(xa[i], 0x3F803F80u, sm); sq = dot2(xa[i], xa[i], sq); gt = dot2(xa[i], ga[i], gt);
        sm = dot2(xb[i], 0x3F803F80u, sm); sq = dot2(xb[i], xb[i], sq); gt = dot2(xb[i], gb[i], gt);
      }
      sm = wavesum(sm); sq = wavesum(sq); gt = wavesum(gt);
      float mean = sm * (1.0f / D);
      float var = fmaxf(sq * (1.0f / D) - mean * mean, 0.0f);
      f32x4 o = {mean, __builtin_amdgcn_rsqf(var + 1e-5f), __builtin_amdgcn_rcpf(1.0f + __expf(-gt)), 0.f};
      __builtin_amdgcn_raw_buffer_store_b128(__builtin_bit_cast(v4u, o), rs, lane == 0 ? 0u : OOB, WS_RS + srw * 16, 16);
    }
    {
      u64 z = (((u64)ep[1] << 32) | ep[0]) + 0x9E3779B97F4A7C15ull;
      z = (z ^ (z >> 30)) * 0xBF58476D1CE4E5B9ull; z = (z ^ (z >> 27)) * 0x94D049BB133111EBull; tag = z ^ (z >> 31);
    }
    unsigned xo[2], wo_[5];
    xo[0] = 81920 + wm * 4096 + r * 128 + ((g ^ (r >> 1)) << 4);
    xo[1] = 81920 + wm * 4096 + r * 128 + (((4 + g) ^ (r >> 1)) << 4);
    {
      int q = (lane >> 2) & 3, pp = lane & 3;
      int swz = ((q >> 1) & 1) | ((g & 1) << 1);
#pragma unroll
      for (int tau = 0; tau < 5; ++tau) {
        int t = tau + wn; if (t >= 5) t -= 5; t += 5 * wn;
        int U = U0 + t;
        wo_[tau] = 81920 + 16384 + (U >> 2) * 8192 + (8 * g + q) * 128 + (((U & 3) ^ swz) << 5) + pp * 8;
      }
    }
#pragma unroll
    for (int t = 0; t < 5; ++t) { acc1[0][t] = f32x4{0, 0, 0, 0}; acc1[1][t] = f32x4{0, 0, 0, 0}; }
    BAR();
    STAMP(1);
    bf16x8 xf0[2], xf1[2], wf[5];
    auto kstep = [&](auto KSN, auto G0, auto GN, auto RD, bf16x8 (&xc)[2], bf16x8 (&xn)[2]) {
      constexpr int ksn = KSN, g0 = G0, gn = GN; constexpr bool rd = RD;
      if constexpr (rd) { xn[0] = *(LAS bf16x8*)(L + xo[ksn]); xn[1] = *(LAS bf16x8*)(L + xo[ksn] + 2048); SB(); }
      sfor<5>([&](auto T) {
        constexpr int t = T;
        acc1[0][t] = mfma(wf[t], xc[0], acc1[0][t]);
        acc1[1][t] = mfma(wf[t], xc[1], acc1[1][t]);
        if constexpr (rd) wf[t] = cat(trread(L + wo_[t] + ksn * 4096), trread(L + wo_[t] + ksn * 4096 + 512));
        if constexpr ((t & 1) == 0 && (t >> 1) < gn) issue(std::integral_constant<int, g0 + (t >> 1)>{});
        SB();
      });
    };
    using I0 = std::integral_constant<int, 0>; using I1 = std::integral_constant<int, 1>; using I2 = std::integral_constant<int, 2>; using I3 = std::integral_constant<int, 3>;
    xf0[0] = *(LAS bf16x8*)(L + xo[0]); xf0[1] = *(LAS bf16x8*)(L + xo[0] + 2048);
#pragma unroll
    for (int t = 0; t < 5; ++t) wf[t] = cat(trread(L + wo_[t]), trread(L + wo_[t] + 512));
    SB();
    kstep(I1{}, I0{}, I0{}, std::true_type{}, xf0, xf1);
    v4u f0a, f0b;
    unsigned rslot = 2;
#pragma unroll 1
    for (int ch = 0; ch < 9; ++ch) {
      if (ch < 2) WAITV(12); else WAITV(5);
      if (ch == 7) {
        if (w == 0) {
          PIN(f0a); PIN(f0b);
          const unsigned tl = (unsigned)tag, th = (unsigned)(tag >> 32);
          bool ok = f0a[0] == tl && f0a[1] == th && f0a[2] == tl && f0a[3] == th && f0b[0] == tl && f0b[1] == th && f0b[2] == tl && f0b[3] == th;
          if (!__all(ok)) {
#pragma unroll 1
            for (int q = 0; q < 4; ++q) poll_flags(p.ws, WS_F0 + (q * 64 + lane) * 8, tag);
          }
          if (b == 0 && lane == 0) set_flag(p.ws, WS_EP, tag);
        }
        sA = p.ws + WS_W2 + ct * 100 * 1024; sB = sA; strA = 40960; strB = 40960;
#pragma unroll
        for (int k = 0; k < 5; ++k) vo[k] = l16 + lw + k * 8192;
      }
      RAWBAR();
      if (w == 0) {
        if (ch == 2) { if (lane == 0) set_flag(p.ws, WS_F0 + b * 8, tag); }
        if (ch == 6) { gload16<0>(f0a, p.ws + WS_F0, l16); gload16<1024>(f0b, p.ws + WS_F0, l16); }
      }
      {
        rslot = (rslot + 1) & 3;
        const int d = rslot == 0 ? -3 * 40960 : 40960;
        xo[0] += d; xo[1] += d;
#pragma unroll
        for (int t = 0; t < 5; ++t) wo_[t] += d;
      }
      SB();
      kstep(I0{}, I0{}, I3{}, std::true_type{}, xf1, xf0);
      kstep(I1{}, I3{}, I2{}, std::true_type{}, xf0, xf1);
    }
    RAWBAR();
    kstep(I0{}, I0{}, I3{}, std::false_type{}, xf1, xf0);
  }
  STAMP(2);
  if (!isw1) {
#pragma unroll
    for (int m = 0; m < 2; ++m) {
      const unsigned so = WS_QKV + ((rt128 * 8 + wm * 2 + m) * 60 + 5 * c) * 1024;
      const int p0 = wk == 0 ? 0 : 3;
#pragma unroll
      for (int pr = 0; pr < 2; ++pr) {
        f32x4 a = acc1[m][2 * pr], bb = acc1[m][2 * pr + 1];
        __builtin_amdgcn_raw_buffer_store_b128(v4u{pk2(a[0], a[1]), pk2(a[2], a[3]), pk2(bb[0], bb[1]), pk2(bb[2], bb[3])}, rs, l16, so + (p0 + pr) * 1024, 16);
      }
      f32x4 a = acc1[m][4];
      __builtin_amdgcn_raw_buffer_store_b64(v2u{pk2(a[0], a[1]), pk2(a[2], a[3])}, rs, l16, so + 2 * 1024 + wk * 8, 16);
    }
  } else {
    LAS float* cs = (LAS float*)(L + 104 * 1024);
    f32x4 st[2];
    gload16<0>(st[0], p.ws + WS_RS + (rt128 * 128 + wm * 32) * 16, (unsigned)(r * 16));
    gload16<256>(st[1], p.ws + WS_RS + (rt128 * 128 + wm * 32) * 16, (unsigned)(r * 16));
    {
      int part = tid / 160, col = tid - part * 160; if (part > 2) { part = 2; }
      float v[7];
      const char* cb = p.ws + WS_CS1 + cc * 640;
      const unsigned vv = (part * 7 * 640 + col) * 4;
      sfor<7>([&](auto I) { constexpr int i = I; gload4<0>(v[i], cb + i * 2560, vv); });
      WAITV(0);
#pragma unroll
      for (int i = 0; i < 7; ++i) PIN(v[i]);
      float s = ((v[0] + v[1]) + (v[2] + v[3])) + (v[4] + v[5]);
      if (part < 2) s += v[6];
      if (tid < 480) cs[tid] = s;
    }
    BAR();
    PIN(st[0]); PIN(st[1]);
    const int delta = cc == 0 ? 1 : cc == 1 ? -1 : cc == 2 ? 2 : -2;
    f32x4 csum[5];
#pragma unroll
    for (int tau = 0; tau < 5; ++tau) {
      int t = tau + wk; if (t >= 5) t -= 5; t += 5 * wk;
      LAS float* q = cs + t * 16 + g * 4;
      csum[tau] = *(LAS f32x4*)(q) + *(LAS f32x4*)(q + 160) + *(LAS f32x4*)(q + 320);
    }
#pragma unroll
    for (int m = 0; m < 2; ++m) {
      const float mean = st[m][0], rstd = st[m][1];
      int drow = rt128 * 128 + wm * 32 + m * 16 + r + delta;
      const bool wrap = (unsigned)drow >= (unsigned)S;
      drow &= (S - 1);
      const unsigned fb = (drow >> 4) * 20 * 1024 + (g * 16 + (drow & 15)) * 16;
      const unsigned so = WS_HS + 5 * cc * 1024;
      unsigned pkd[5][2];
#pragma unroll
      for (int tau = 0; tau < 5; ++tau) {
        float h[4];
#pragma unroll
        for (int e = 0; e < 4; ++e) h[e] = gelu_t(rstd * (acc1[m][tau][e] - mean * csum[tau][e]));
        pkd[tau][0] = wrap ? 0u : pk2(h[0], h[1]);
        pkd[tau][1] = wrap ? 0u : pk2(h[2], h[3]);
      }
      const int p0 = wk == 0 ? 0 : 3;
#pragma unroll
      for (int pr = 0; pr < 2; ++pr)
        __builtin_amdgcn_raw_buffer_store_b128(v4u{pkd[2 * pr][0], pkd[2 * pr][1], pkd[2 * pr + 1][0], pkd[2 * pr + 1][1]}, rs, fb, so + (p0 + pr) * 1024, 16);
      __builtin_amdgcn_raw_buffer_store_b64(v2u{pkd[4][0], pkd[4][1]}, rs, fb, so + 2 * 1024 + wk * 8, 16);
    }
  }
  WAITV(0);
  BAR();
  if (tid == 0) set_flag(p.ws, WS_F1 + (rt128 * 16 + c) * 128, tag);
  STAMP(3);
#pragma unroll
  for (int i = 0; i < 2; ++i) {
    int q = w + 8 * i; if (q >= 10) q = 9;
    int gi = q * 64 + lane; int row = (gi * 6554) >> 16, cq = gi - row * 10;
    glds<0>((const char*)p.x + (size_t)rt64 * 64 * 1280 + ct * 160, row * 1280 + cq * 16, L_MISC + q * 1024);
  }
  if (w == 0) {
    int fi;
    const int own = rt64 >> 1;
    if (lane < 4) fi = own * 16 + 12 + lane;
    else if (lane < 6) fi = (rt64 & 1) ? ((own + 1) & 15) * 16 + 13 + 2 * (lane - 4) : ((own + 15) & 15) * 16 + 12 + 2 * (lane - 4);
    else if (lane < 9) fi = (rb >> 2) * 16 + 4 * (lane - 6) + hg;
    else if (lane < 11 && (rb & 3) == 0 && rb > 0) fi = ((rb >> 2) - 1) * 16 + 4 * (lane - 8) + hg;
    else fi = own * 16 + 12;
    poll_flags(p.ws, WS_F1 + fi * 128, tag);
  }
  const unsigned bA0 = l16 + wk * 5120, bA1 = bA0 + 61440, bA2 = bA0 + 122880;
  const int cg = c;
  const int st3 = 27 * (cg >> 2) + ((cg & 3) == 3 ? 20 : 7 * (cg & 3));
  const char* sb3 = p.ws + WS_W3 + cg * 140 * 1024 + lw;
  const char* sbo = p.ws + WS_WO + ct * 100 * 1024 + lw;
  const char* sb4 = p.ws + WS_W4 + ct * 270 * 1024 + lw;
  RAWBAR();
  STAMP(4);
  f32x4 acc[5];
  unsigned x1p[3][2];
#define BRD(dst, s) do { if constexpr ((s) < 60) ADSR(dst, bA0, (s) * 1024); else if constexpr ((s) < 120) ADSR(dst, bA1, ((s) - 60) * 1024); else ADSR(dst, bA2, ((s) - 120) * 1024); } while (0)
  {
    bf16x8 af[10];
    const char* ap = p.ws + WS_HS + (size_t)((rt64 * 4 + wm) * 20 + wk) * 1024;
    sfor<10>([&](auto I) { constexpr int i = I; gload16<(i & 1) * 2048>(af[i], ap + (i >> 1) * 4096, l16); });
    int art, apb;
    if (w < 2) { art = 2 * rb + w; apb = 0; } else if (w < 5) { art = 2 * rb - 1 + (w - 2); apb = 20; } else { art = 2 * rb - 1 + (w - 5); apb = 40; }
    if (art < 0) art = 0;
    const char* asb = p.ws + WS_QKV + (size_t)(art * 60 + apb + 5 * hg) * 1024;
#pragma unroll
    for (int t = 0; t < 5; ++t) acc[t] = f32x4{0, 0, 0, 0};
    bf16x8 bfr[5];
    sfor<5>([&](auto T) { constexpr int t = T; BRD(bfr[t], t); });
    sfor<10>([&](auto I) {
      constexpr int i = I;
      constexpr int ng = i < 5 ? i : 5;
      WAITV_R(9 - i + ng, af[i]);
      if constexpr (i == 5) RAWBAR();
      sfor<5>([&](auto T) {
        constexpr int t = T;
        if constexpr (i < 9) WAITL(4); else WAITL(4 - t);
        AMFMA(acc[t], bfr[t], af[i]);
        if constexpr (i < 9) BRD(bfr[t], (i + 1) * 10 + t);
        if constexpr (t == 2 && i < 5) { if constexpr (i == 0) glds<0>(asb, l16, (100 + w * 5) * 1024); else if constexpr (i < 4) glds_same_m0<i * 1024>(asb, l16); else glds<0>(asb + 4096, l16, (104 + w * 5) * 1024); }
      });
    });
    MFMA_FENCE5(acc);
    STAMP(5);
    reduce_wk(L, wm, wk, lane, acc);
    const int nv = wk == 0 ? 3 : 2;
    float sum = 0, sq = 0;
#pragma unroll
    for (int i = 0; i < 3; ++i) {
      int tl = ntile_of(ct, wk, i) - 5 * ct;
      v2u xr = *(LAS v2u*)(L + L_MISC + (wm * 16 + r) * 160 + tl * 32 + g * 8);
      x1p[i][0] = pk2(bflo(xr[0]) + acc[i][0], bfhi(xr[0]) + acc[i][1]); x1p[i][1] = pk2(bflo(xr[1]) + acc[i][2], bfhi(xr[1]) + acc[i][3]);
      if (i < nv) {
        sum = dot2(x1p[i][0], 0x3F803F80u, sum); sum = dot2(x1p[i][1], 0x3F803F80u, sum);
        sq = dot2(x1p[i][0], x1p[i][0], sq); sq = dot2(x1p[i][1], x1p[i][1], sq);
      }
    }
    const unsigned so = WS_X1 + (rt64 * 4 + wm) * 20 * 1024;
    const int nt0 = ntile_of(ct, wk, 0), nt2 = ntile_of(ct, 0, 2);
    __builtin_amdgcn_raw_buffer_store_b128(v4u{x1p[0][0], x1p[0][1], x1p[1][0], x1p[1][1]}, rs, l16, so + (nt0 >> 1) * 1024, 16);
    __builtin_amdgcn_raw_buffer_store_b64(v2u{x1p[2][0], x1p[2][1]}, rs, wk == 0 ? l16 : OOB, so + (nt2 >> 1) * 1024 + (nt2 & 1) * 8, 16);
    sum = xsum4(sum); sq = xsum4(sq);
    __builtin_amdgcn_raw_buffer_store_b64(__builtin_bit_cast(v2u, f32x2{sum, sq}), rs, g == 0 ? r * 128 : OOB, WS_ST + ((rt64 * 64 + wm * 16) * 16 + ct * 2 + wk) * 8, 16);
  }
  sfor<4>([&](auto I) { constexpr int i = I; bload<24 + 8 * i, 24 + 8 * i>(sb3, l16, lw); });
  WAITV(4);
  BAR();
  if (tid == 0) set_flag(p.ws, WS_F2 + (rt64 * 8 + ct) * 128, tag);
  STAMP(6);
  {
    LAS char* A = L + 100 * 1024;
    const int hh = w;
    auto qaddr = [&](int Q, int row) { return (Q >> 3) * 1024 + ((Q & 3) * 16 + row) * 16 + ((Q >> 2) & 1) * 8; };
    const int a0 = qaddr(5 * hh + g, r), a1 = qaddr(5 * hh + 4, r);
    bf16x8 kf[3];
#pragma unroll
    for (int t = 0; t < 3; ++t) kf[t] = cat(*(LAS bf16x4*)(A + (10 + 5 * t) * 1024 + a0), *(LAS bf16x4*)(A + (10 + 5 * t) * 1024 + a1));
    const int q4 = (lane >> 2) & 3, pp = lane & 3;
    const int v0 = qaddr(5 * hh + pp, 4 * g + q4), v1 = qaddr(5 * hh + 4, 4 * g + q4);
    bf16x4 vt0[3], vt1[3], qlo[2], qhi[2];
#pragma unroll
    for (int t = 0; t < 3; ++t) { vt0[t] = trread(A + (25 + 5 * t) * 1024 + v0); vt1[t] = trread(A + (25 + 5 * t) * 1024 + v1); }
#pragma unroll
    for (int qb = 0; qb < 2; ++qb) { qlo[qb] = *(LAS bf16x4*)(A + (5 * qb) * 1024 + a0); qhi[qb] = *(LAS bf16x4*)(A + (5 * qb) * 1024 + a1); }
    sfor<3>([&](auto I) { constexpr int i = I; bload<8 * i, 8 * i>(sb3, l16, lw); });
    LAS char* Lo = L + L_MISC + 10240;
    const int dd = r - 4 * g;
#pragma unroll
    for (int qb = 0; qb < 2; ++qb) {
      bf16x4 qh = qhi[qb];
      if (g != 0) qh = bf16x4{0, 0, 0, 0};
      bf16x8 qf = cat(qlo[qb], qh);
      f32x4 z = {0, 0, 0, 0};
      f32x4 sp = mfma(kf[qb], qf, z), sc = mfma(kf[qb + 1], qf, z);
      const bool prev_ok = (rb | qb) != 0;
      const float NEG = -__builtin_inff();
      float mx = NEG;
#pragma unroll
      for (int e = 0; e < 4; ++e) {
        int dc = dd - e;
        bool even = (dc & 1) == 0;
        bool vc = even && dc >= 0;
        bool vp = even && dc < 0 && prev_ok;
        sp[e] = vp ? sp[e] : NEG; sc[e] = vc ? sc[e] : NEG;
        mx = fmaxf(mx, fmaxf(sp[e], sc[e]));
      }
      mx = xmax4(mx);
      const float cexp = 0.22360679774997896f * 1.4426950408889634f;
      const float mc = mx * cexp;
      float sum = 0;
#pragma unroll
      for (int e = 0; e < 4; ++e) {
        sp[e] = __builtin_amdgcn_exp2f(__builtin_fmaf(sp[e], cexp, -mc)); sc[e] = __builtin_amdgcn_exp2f(__builtin_fmaf(sc[e], cexp, -mc));
        sum += sp[e] + sc[e];
      }
      sum = xsum4(sum);
      v4u pu = {pk2(sp[0], sp[1]), pk2(sp[2], sp[3]), pk2(sc[0], sc[1]), pk2(sc[2], sc[3])};
      bf16x8 pf = __builtin_bit_cast(bf16x8, pu);
      f32x4 o0 = mfma(cat(vt0[qb], vt0[qb + 1]), pf, z), o1 = mfma(cat(vt1[qb], vt1[qb + 1]), pf, z);
      float inv = __builtin_amdgcn_rcpf(sum);
      *(LAS v2u*)(Lo + qb * 5120 + a0) = v2u{pk2(o0[0] * inv, o0[1] * inv), pk2(o0[2] * inv, o0[3] * inv)};
      if (g == 0) *(LAS v2u*)(Lo + qb * 5120 + a1) = v2u{pk2(o1[0] * inv, o1[1] * inv), pk2(o1[2] * inv, o1[3] * inv)};
      if (qb == 0) sfor<3>([&](auto I) { constexpr int i = I; bload<56 + 8 * i, 56 + 8 * i>(sb3, l16, lw); });
    }
    sfor<2>([&](auto I) { constexpr int i = I; bload<80 + 8 * i, 80 + 8 * i>(sb3, l16, lw); });
    BAR();
#pragma unroll
    for (int i = 0; i < 2; ++i) {
      int f = w + 8 * i; bool ok = f < 10; if (!ok) f = 9;
      v4u v = *(LAS v4u*)(Lo + f * 1024 + l16);
      int fr = f >= 5 ? 1 : 0;
      __builtin_amdgcn_raw_buffer_store_b128(v, rs, ok ? l16 : OOB, WS_O + ((2 * rb + fr) * 20 + 5 * hg + f - 5 * fr) * 1024, 16);
    }
  }
  sfor<5>([&](auto I) { constexpr int i = I; bload<96 + 8 * i, 96 + 8 * i>(sb3, l16, lw); });
  WAITV(5);
  RAWBAR();
  if (tid == 0) set_flag(p.ws, WS_FA + (rb * 4 + hg) * 128, tag);
  STAMP(7);
  if (w < 4) bload<136, 136>(sb3, l16, lw);
  f32x4 av[3];
  {
    LAS float* csp = (LAS float*)(L + L_MISC);
    {
      int part = tid / 112, col = tid - part * 112; if (part > 3) part = 3;
      int tau = col >> 4; int t = tau + (st3 & 1); if (t >= 7) t -= 7;
      float v[5];
      const unsigned vv = (part * 5 * 1728 + (st3 + t) * 16 + (col & 15)) * 4;
      sfor<5>([&](auto I) { constexpr int i = I; gload4<0>(v[i], p.ws + WS_CS3 + i * 6912, vv); });
      WAITV(0);
#pragma unroll
      for (int i = 0; i < 5; ++i) PIN(v[i]);
      if (tid < 448) csp[tid] = ((v[0] + v[1]) + (v[2] + v[3])) + v[4];
    }
    if (w == 0) poll_flags(p.ws, WS_F2 + (rt128 * 16 + (lane & 15)) * 128, tag);
    BAR();
    STAMP(8);
    const int rt = rt128 * 8 + w;
    constexpr int PD = 8;
    bf16x8 af[20];
    const char* ap = p.ws + WS_X1 + (size_t)rt * 20 * 1024;
    sfor<PD>([&](auto I) { constexpr int i = I; gload16<(i & 3) * 1024>(af[i], ap + (i >> 2) * 4096, l16); });
    f32x4 acc3[7], stp[2];
#pragma unroll
    for (int t = 0; t < 7; ++t) acc3[t] = f32x4{0, 0, 0, 0};
    bf16x8 bfr[7];
    const unsigned c0 = l16, c1 = l16 + 61440, c2 = l16 + 122880;
#define BRD3(dst, s) do { if constexpr ((s) < 60) ADSR(dst, c0, (s) * 1024); else if constexpr ((s) < 120) ADSR(dst, c1, ((s) - 60) * 1024); else ADSR(dst, c2, ((s) - 120) * 1024); } while (0)
    sfor<7>([&](auto T) { constexpr int t = T; BRD3(bfr[t], t); });
    u64 fav;
    sfor<20>([&](auto I) {
      constexpr int ks = I;
      constexpr auto nA = [](int s) { return s < 12 ? 1 : s < 14 ? 1 : 0; };
      constexpr auto nB = [](int s) { return (s >= 5 && s <= 8) || (s >= 10 && s <= 13) || (s >= 15 && s <= 19) ? 1 : 0; };
      constexpr int cnt = [&]() {
        int n = 0;
        if (ks < PD) { n = PD - 1 - ks; for (int s = 0; s < ks; ++s) n += nA(s) + nB(s); }
        else { int s0 = ks - PD; n = nB(s0); for (int s = s0 + 1; s < ks; ++s) n += nA(s) + nB(s); }
        return n; }();
      if constexpr (ks == 5 || ks == 10 || ks == 15) RAWBAR();
      WAITV_R(cnt, af[ks]);
      sfor<7>([&](auto T) {
        constexpr int t = T;
        if constexpr (ks < 19) WAITL(6); else WAITL(6 - t);
        AMFMA(acc3[t], bfr[t], af[ks]);
        if constexpr (ks < 19) BRD3(bfr[t], (ks + 1) * 7 + t);
        if constexpr (t == 1 && ks < 12) gload16<((ks + PD) & 3) * 1024>(af[ks + PD], ap + ((ks + PD) >> 2) * 4096, l16);
        if constexpr (t == 1 && (ks == 12 || ks == 13)) gload16<(ks - 12) * 16>(stp[ks - 12], p.ws + WS_ST + (size_t)rt * 16 * 128, (unsigned)(r * 128 + g * 32));
        if constexpr (t == 4 && nB(ks)) {
          constexpr int gi = ks <= 8 ? ks - 5 : ks <= 13 ? ks - 6 : ks - 7;
          if constexpr (gi < 12) bload<8 * gi, 8 * gi>(sbo, l16, lw); else bload<92, 92>(sbo, l16, lw);
        }
        if constexpr (t == 6 && ks == 14) { if (w == 0) gload8(fav, p.ws + WS_FA, (unsigned)((rt64 * 8 + (lane & 7)) * 128)); }
      });
    });
    asm volatile("s_nop 7\n\ts_nop 1" : "+v"(acc3[0]), "+v"(acc3[1]), "+v"(acc3[2]), "+v"(acc3[3]), "+v"(acc3[4]), "+v"(acc3[5]), "+v"(acc3[6]));
    STAMP(9);
    WAITV(5);
    if (w == 0) {
      PIN(fav);
      if (!__all(fav == tag)) poll_flags(p.ws, WS_FA + (rt64 * 8 + (lane & 7)) * 128, tag);
    }
    RAWBAR();
    bf16x8 ao[10];
    const char* op = p.ws + WS_O + (size_t)((rt64 * 4 + wm) * 20 + wk) * 1024;
    PIN(stp[0]); PIN(stp[1]);
    float sum = xsum4((stp[0][0] + stp[0][2]) + (stp[1][0] + stp[1][2]));
    float sq = xsum4((stp[0][1] + stp[0][3]) + (stp[1][1] + stp[1][3]));
    const float mean = sum * (1.0f / D);
    const float rstd = __builtin_amdgcn_rsqf(fmaxf(sq * (1.0f / D) - mean * mean, 0.f) + 1e-5f);
    unsigned pk[7][2];
    sfor<7>([&](auto T) {
      constexpr int t = T;
      LAS float* q = csp + t * 16 + g * 4;
      f32x4 cv = (*(LAS f32x4*)(q) + *(LAS f32x4*)(q + 112)) + (*(LAS f32x4*)(q + 224) + *(LAS f32x4*)(q + 336));
      if constexpr (t < 5) { gload16<0>(ao[2 * t], op + t * 4096, l16); gload16<2048>(ao[2 * t + 1], op + t * 4096, l16); }
      else if constexpr (t == 5) { bload<0, 100>(sb4, l16, lw); bload<8, 108>(sb4, l16, lw); }
      float h[4];
#pragma unroll
      for (int e = 0; e < 4; ++e) h[e] = gelu_t(rstd * (acc3[t][e] - mean * cv[e]));
      pk[t][0] = pk2(h[0], h[1]); pk[t][1] = pk2(h[2], h[3]);
    });
    const unsigned so = WS_U + rt * 54 * 1024;
    const int pb = (st3 + (st3 & 1)) >> 1;
#pragma unroll
    for (int pr = 0; pr < 3; ++pr) __builtin_amdgcn_raw_buffer_store_b128(v4u{pk[2 * pr][0], pk[2 * pr][1], pk[2 * pr + 1][0], pk[2 * pr + 1][1]}, rs, l16, so + (pb + pr) * 1024, 16);
    const int nts = (st3 & 1) ? st3 : st3 + 6;
    __builtin_amdgcn_raw_buffer_store_b64(v2u{pk[6][0], pk[6][1]}, rs, l16, so + (nts >> 1) * 1024 + (nts & 1) * 8, 16);
    sfor<3>([&](auto I) { constexpr int i = I; bload<16 + 8 * i, 116 + 8 * i>(sb4, l16, lw); });
    WAITV(3);
    BAR();
    if (tid == 0) set_flag(p.ws, WS_F3 + (rt128 * 16 + cg) * 128, tag);
    STAMP(10);
#pragma unroll
    for (int t = 0; t < 5; ++t) acc[t] = f32x4{0, 0, 0, 0};
    bf16x8 bo[5];
    sfor<5>([&](auto T) { constexpr int t = T; BRD(bo[t], t); });
    sfor<10>([&](auto I) {
      constexpr int i = I;
      PIN(ao[i]);
      if constexpr (i == 3 || i == 6) RAWBAR();
      sfor<5>([&](auto T) {
        constexpr int t = T;
        if constexpr (i < 9) WAITL(4); else WAITL(4 - t);
        AMFMA(acc[t], bo[t], ao[i]);
        if constexpr (i < 9) BRD(bo[t], (i + 1) * 10 + t);
        if constexpr (t == 2 && i >= 3) bload<40 + 8 * (i - 3), 8 * (i - 3)>(sb4, l16, lw);
      });
    });
    MFMA_FENCE5(acc);
    reduce_wk(L + L_MISC, wm, wk, lane, acc);
    sfor<5>([&](auto I) { constexpr int i = I; bload<96 + 8 * i, 56 + 8 * i>(sb4, l16, lw); });
    if (w < 4) bload<136, 96>(sb4, l16, lw);
    av[0] = acc[0]; av[1] = acc[1]; av[2] = acc[2];
    STAMP(11);
  }
  {
    f32x4 rsv;
    gload16<0>(rsv, p.ws + WS_RS + (rt64 * 64 + wm * 16) * 16, (unsigned)(r * 16));
    WAITV(0);
    if (w == 0) poll_flags(p.ws, WS_F3 + ((rt64 >> 1) * 16 + (lane & 15)) * 128, tag);
    BAR();
    STAMP(12);
    constexpr int PD = 10;
    bf16x8 af[27];
    const char* ap = p.ws + WS_U + (size_t)((rt64 * 4 + wm) * 54 + wk) * 1024;
    sfor<PD>([&](auto I) { constexpr int i = I; gload16<(i & 1) * 2048>(af[i], ap + (i >> 1) * 4096, l16); });
#pragma unroll
    for (int t = 0; t < 5; ++t) acc[t] = f32x4{0, 0, 0, 0};
    bf16x8 bfr[5];
    sfor<5>([&](auto T) { constexpr int t = T; BRD(bfr[t], 100 + t); });
    sfor<27>([&](auto I) {
      constexpr int i = I;
      constexpr auto nA = [](int s) { return s + PD < 27 ? 1 : 0; };
      constexpr auto nB = [](int s) { return (s >= 2 && s <= 18) ? 1 : 0; };
      constexpr int cntA = [&]() {
        int n = 0;
        if (i < PD) { n = PD - 1 - i; for (int s = 0; s < i; ++s) n += nA(s) + nB(s); }
        else { int s0 = i - PD; n = nB(s0); for (int s = s0 + 1; s < i; ++s) n += nA(s) + nB(s); }
        return n; }();
      constexpr int qneed = (10 * (i + 3) - 140 + 7) / 8 - 1;
      constexpr int cntB = [&]() {
        if (10 * (i + 3) <= 140) return 63;
        int q = qneed > 16 ? 16 : qneed; int s0 = 2 + q; int n = 0;
        for (int s = s0 + 1; s < i; ++s) n += nA(s) + nB(s);
        return n; }();
      if constexpr ((i & 1) == 0 && i >= 2 && i <= 24) { if constexpr (cntB < 63) WAITV(cntB); RAWBAR(); }
      WAITV_R(cntA, af[i]);
      sfor<5>([&](auto T) {
        constexpr int t = T;
        if constexpr (i < 26) WAITL(4); else WAITL(4 - t);
        AMFMA(acc[t], bfr[t], af[i]);
        if constexpr (i < 26) BRD(bfr[t], (100 + 10 * (i + 1)) % 140 + t);
        if constexpr (t == 0 && nA(i)) gload16<((i + PD) & 1) * 2048>(af[i + PD], ap + ((i + PD) >> 1) * 4096, l16);
        if constexpr (t == 3 && nB(i)) {
          constexpr int q = i - 2;
          constexpr int f = q < 16 ? 140 + 8 * q : 262;
          constexpr int sl = (100 + f) % 140;
          if constexpr (sl + 7 < 140) bload<f, sl>(sb4, l16, lw);
          else glds<0>(sb4 + (size_t)f * 1024, l16, (unsigned)((sl + w >= 140 ? sl + w - 140 : sl + w) * 1024));
        }
      });
    });
    MFMA_FENCE5(acc);
    STAMP(13);
    reduce_wk(L + L_MISC, wm, wk, lane, acc);
    PIN(rsv);
    const float gate = rsv[2];
    const int nv = wk == 0 ? 3 : 2;
#pragma unroll
    for (int i = 0; i < 3; ++i) if (i < nv) {
      float b0 = bflo(x1p[i][0]) + acc[i][0], b1 = bfhi(x1p[i][0]) + acc[i][1], b2 = bflo(x1p[i][1]) + acc[i][2], b3 = bfhi(x1p[i][1]) + acc[i][3];
      float o0 = gate * av[i][0] + (1.f - gate) * b0, o1 = gate * av[i][1] + (1.f - gate) * b1, o2 = gate * av[i][2] + (1.f - gate) * b2, o3 = gate * av[i][3] + (1.f - gate) * b3;
      int tl = ntile_of(ct, wk, i) - 5 * ct;
      *(LAS v2u*)(L + (wm * 16 + r) * 176 + tl * 32 + g * 8) = v2u{pk2(o0, o1), pk2(o2, o3)};
    }
    BAR();
#pragma unroll
    for (int k = 0; k < 2; ++k) {
      int i = tid + 512 * k;
      if (i < 640) {
        int rr = (i * 6554) >> 16, cq = i - rr * 10;
        v4u v = *(LAS v4u*)(L + rr * 176 + cq * 16);
        *(v4u*)((char*)p.out + (size_t)(rt64 * 64 + rr) * 1280 + ct * 160 + cq * 16) = v;
      }
    }
    STAMP(14);
  }
}

extern "C" void kernel_launch(void* const* d_in, const int* in_sizes, int n_in, void* d_out, int out_size, void* d_ws, size_t ws_size, hipStream_t stream) {
  Params p;
  p.x = (const u16*)d_in[0]; p.wqkv = (const u16*)d_in[1]; p.wo = (const u16*)d_in[2];
  p.w1 = (const u16*)d_in[4]; p.w2 = (const u16*)d_in[5]; p.w3 = (const u16*)d_in[6]; p.w4 = (const u16*)d_in[7]; p.gw = (const u16*)d_in[8];
  p.out = (u16*)d_out; p.ws = (char*)d_ws;
  const int lds = 163840;
  (void)hipFuncSetAttribute((const void*)mega, hipFuncAttributeMaxDynamicSharedMemorySize, lds);
  mega<<<256, 512, lds, stream>>>(p);
}
```

```cpp
#include <hip/hip_runtime.h>
#include <stdint.h>
#include <type_traits>

typedef unsigned short u16;
typedef unsigned long long u64;
typedef short bf16x8 __attribute__((ext_vector_type(8)));
typedef short bf16x4 __attribute__((ext_vector_type(4)));
typedef float f32x4 __attribute__((ext_vector_type(4)));
typedef float f32x2 __attribute__((ext_vector_type(2)));
typedef __bf16 bfv2 __attribute__((ext_vector_type(2)));
typedef unsigned v4u __attribute__((ext_vector_type(4)));
typedef unsigned v2u __attribute__((ext_vector_type(2)));
#define LAS __attribute__((address_space(3)))
#define GAS __attribute__((address_space(1)))
#define DEV __device__ __forceinline__
#define RLX_AGENT __ATOMIC_RELAXED, __HIP_MEMORY_SCOPE_AGENT

constexpr int S = 2048, D = 640, F = 1728;
constexpr unsigned KB = 1024;
constexpr unsigned WS_W4 = 0;
constexpr unsigned WS_QKV = WS_W4 + 2960 * KB;
constexpr unsigned WS_HS = WS_QKV + 7680 * KB;
constexpr unsigned WS_O = WS_HS + 2560 * KB;
constexpr unsigned WS_X1 = WS_O + 2560 * KB;
constexpr unsigned WS_U = WS_X1 + 2560 * KB;
constexpr unsigned WS_ST = WS_U + 6912 * KB;
constexpr unsigned WS_RS = WS_ST + 256 * KB;
constexpr unsigned WS_GT = WS_RS + 32 * KB;
constexpr unsigned WS_CS = WS_GT + 32 * KB;
constexpr unsigned WS_EP = WS_CS + 20 * KB;
constexpr unsigned WS_FW = WS_EP + 1 * KB;
constexpr unsigned WS_FQ = WS_FW + 2 * KB;
constexpr unsigned WS_FH = WS_FQ + 2 * KB;
constexpr unsigned WS_F2 = WS_FH + 16 * KB;
constexpr unsigned WS_FA = WS_F2 + 32 * KB;
constexpr unsigned WS_F3 = WS_FA + 32 * KB;
constexpr unsigned WS_END = WS_F3 + 32 * KB;
constexpr unsigned OOB = 0x40000000u;
constexpr unsigned L_JOB = 104 * KB, L_ATT = 70 * KB, L_CS3 = 156 * KB;
constexpr int B0 = 110;
constexpr int RING = 150;

DEV f32x4 mfma(bf16x8 a, bf16x8 b, f32x4 c) { return __builtin_amdgcn_mfma_f32_16x16x32_bf16(a, b, c, 0, 0, 0); }
DEV unsigned pk2(float a, float b) { f32x2 v = {a, b}; bfv2 r = __builtin_convertvector(v, bfv2); return __builtin_bit_cast(unsigned, r); }
DEV float bflo(unsigned d) { return __uint_as_float(d << 16); }
DEV float bfhi(unsigned d) { return __uint_as_float(d & 0xffff0000u); }
DEV float dot2(unsigned a, unsigned b, float c) { return __builtin_amdgcn_fdot2_f32_bf16(__builtin_bit_cast(bfv2, a), __builtin_bit_cast(bfv2, b), c, false); }
DEV float gelu_s(float a, float k1, float k2, float sd) {
  float t = a * __builtin_fmaf(a * a, k2, k1);
  float e = __builtin_amdgcn_exp2f(t);
  return a * __builtin_amdgcn_rcpf(__builtin_fmaf(e, sd, sd));
}
constexpr float GC1 = -2.0f * 0.7978845608028654f * 1.4426950408889634f, GC2 = GC1 * 0.044715f;
DEV void glds(const void* sbase, unsigned voff, unsigned lds) {
  asm volatile("s_mov_b32 m0, %2\n\ts_nop 0\n\tglobal_load_lds_dwordx4 %0, %1 sc1" ::"v"(voff), "s"(sbase), "s"(lds) : "memory");
}
template <int IMM> DEV void glds_same_m0(const void* sbase, unsigned voff) {
  asm volatile("global_load_lds_dwordx4 %0, %1 offset:%2 sc1" ::"v"(voff), "s"(sbase), "n"(IMM) : "memory");
}
template <int IMM, typename T> DEV void gload16(T& d, const void* sbase, unsigned voff) {
  asm volatile("global_load_dwordx4 %0, %1, %2 offset:%3 sc1" : "=v"(d) : "v"(voff), "s"(sbase), "n"(IMM) : "memory");
}
template <int IMM, typename T> DEV void gload8(T& d, const void* sbase, unsigned voff) {
  asm volatile("global_load_dwordx2 %0, %1, %2 offset:%3 sc1" : "=v"(d) : "v"(voff), "s"(sbase), "n"(IMM) : "memory");
}
template <int IMM, typename T> DEV void gload4(T& d, const void* sbase, unsigned voff) {
  asm volatile("global_load_dword %0, %1, %2 offset:%3 sc1" : "=v"(d) : "v"(voff), "s"(sbase), "n"(IMM) : "memory");
}
DEV bf16x8 cat(bf16x4 a, bf16x4 b) { return __builtin_shufflevector(a, b, 0, 1, 2, 3, 4, 5, 6, 7); }
#define WAITV(n) asm volatile("s_waitcnt vmcnt(%0)" ::"n"(n) : "memory")
#define WAITV_R(n, r) asm volatile("s_waitcnt vmcnt(%1)" : "+v"(r) : "n"(n) : "memory")
#define PIN(r) asm volatile("" : "+v"(r))
#define BAR() asm volatile("s_waitcnt lgkmcnt(0)\n\ts_barrier" ::: "memory")
#define RAWBAR() asm volatile("s_barrier" ::: "memory")
#define SB() __builtin_amdgcn_sched_barrier(0)
#define AMFMA(acc, a, b) asm volatile("v_mfma_f32_16x16x32_bf16 %0, %1, %2, %0" : "+v"(acc) : "v"(a), "v"(b))
#define AMFMA0(acc, a, b) asm volatile("v_mfma_f32_16x16x32_bf16 %0, %1, %2, 0" : "=&v"(acc) : "v"(a), "v"(b))
#define ADSR(dst, addr, off) asm volatile("ds_read_b128 %0, %1 offset:%2" : "=v"(dst) : "v"(addr), "n"(off))
#define ADSR8(dst, addr, off) asm volatile("ds_read_b64 %0, %1 offset:%2" : "=v"(dst) : "v"(addr), "n"(off))
#define ADSTR(dst, addr, off) asm volatile("ds_read_b64_tr_b16 %0, %1 offset:%2" : "=v"(dst) : "v"(addr), "n"(off))
#define WAITL(n) asm volatile("s_waitcnt lgkmcnt(%0)" ::"n"(n))
#define TRW(var, R0, R1, R2, R3, addr, off) asm volatile("ds_read_b64_tr_b16 v[" #R0 ":" #R1 "], %1 offset:%2\n\tds_read_b64_tr_b16 v[" #R2 ":" #R3 "], %1 offset:%3" : "={v[" #R0 ":" #R3 "]}"(var) : "v"(addr), "n"(off), "n"((off) + 512))
#define MMW(first, acc, var, R0, R3, x) do { if constexpr (first) asm volatile("v_mfma_f32_16x16x32_bf16 %0, %1, %2, 0" : "=&v"(acc) : "{v[" #R0 ":" #R3 "]}"(var), "v"(x)); \
    else asm volatile("v_mfma_f32_16x16x32_bf16 %0, %1, %2, %0" : "+v"(acc) : "{v[" #R0 ":" #R3 "]}"(var), "v"(x)); } while (0)
#define TRP(R0, R1, R2, R3) asm volatile("ds_read_b64_tr_b16 v[" #R0 ":" #R1 "], %1 offset:%2\n\tds_read_b64_tr_b16 v[" #R2 ":" #R3 "], %1 offset:%3" : "={v[" #R0 ":" #R3 "]}"(d) : "v"(a), "n"(O1), "n"(O2))
template <int T, int O1, int O2> DEV void trb(bf16x8& d, unsigned a) {
  if constexpr (T == 0) TRP(100, 101, 102, 103); else if constexpr (T == 1) TRP(104, 105, 106, 107); else if constexpr (T == 2) TRP(108, 109, 110, 111); else if constexpr (T == 3) TRP(112, 113, 114, 115);
  else if constexpr (T == 4) TRP(116, 117, 118, 119); else if constexpr (T == 5) TRP(120, 121, 122, 123); else TRP(124, 125, 126, 127);
}
template <int T, bool FI> DEV void mmb(f32x4& acc, bf16x8& wv, bf16x8& x) {
  if constexpr (T == 0) MMW(FI, acc, wv, 100, 103, x); else if constexpr (T == 1) MMW(FI, acc, wv, 104, 107, x); else if constexpr (T == 2) MMW(FI, acc, wv, 108, 111, x); else if constexpr (T == 3) MMW(FI, acc, wv, 112, 115, x);
  else if constexpr (T == 4) MMW(FI, acc, wv, 116, 119, x); else if constexpr (T == 5) MMW(FI, acc, wv, 120, 123, x); else MMW(FI, acc, wv, 124, 127, x);
}
template <typename T> DEV void pin(T& v) { PIN(v); }
template <int OFF, typename T> DEV void dstr(T& d, unsigned a) { ADSTR(d, a, OFF); }
template <int OFF> DEV void dsw16(unsigned a, v4u v) { asm volatile("ds_write_b128 %0, %1 offset:%2" ::"v"(a), "v"(v), "n"(OFF) : "memory"); }
template <int OFF, typename T> DEV void dsr16(T& d, unsigned a) { ADSR(d, a, OFF); }
DEV void bstore16(v4u v, v4u rq, unsigned voff, unsigned soff) {
  asm volatile("buffer_store_dwordx4 %0, %1, %2, %3 offen sc1\n\ts_nop 1" ::"v"(v), "v"(voff), "s"(rq), "s"(soff) : "memory");
}
#define BSTORE16(v, voff, soff) bstore16(v, rq, voff, soff)

#define STAMP(i) do {} while (0)

struct Params {
  const u16 *x, *wqkv, *wo, *w1, *w2, *w3, *w4, *gw;
  u16* out;
  char* ws;
};

template <int V> using IC = std::integral_constant<int, V>;
template <int N, typename Fn> DEV void sfor_impl(Fn&& f) { if constexpr (N > 0) { sfor_impl<N - 1>(f); f(IC<N - 1>{}); } }
template <int N, typename Fn> DEV void sfor(Fn&& f) { sfor_impl<N>(f); }

DEV float xsum4(float v) {
  unsigned u = __float_as_uint(v);
  auto a = __builtin_amdgcn_permlane16_swap(u, u, false, false);
  v = __uint_as_float(a[0]) + __uint_as_float(a[1]);
  u = __float_as_uint(v);
  auto b = __builtin_amdgcn_permlane32_swap(u, u, false, false);
  return __uint_as_float(b[0]) + __uint_as_float(b[1]);
}
DEV float xmax4(float v) {
  unsigned u = __float_as_uint(v);
  auto a = __builtin_amdgcn_permlane16_swap(u, u, false, false);
  v = fmaxf(__uint_as_float(a[0]), __uint_as_float(a[1]));
  u = __float_as_uint(v);
  auto b = __builtin_amdgcn_permlane32_swap(u, u, false, false);
  return fmaxf(__uint_as_float(b[0]), __uint_as_float(b[1]));
}
DEV v2u sw16(unsigned a, unsigned b) { auto t = __builtin_amdgcn_permlane16_swap(a, b, false, false); return v2u{t[0], t[1]}; }
DEV unsigned hi32(unsigned u) { auto b = __builtin_amdgcn_permlane32_swap(u, u, false, false); return b[1]; }
template <int CTRL> DEV float dpp_add(float v) { return v + __uint_as_float(__builtin_amdgcn_update_dpp(0, __float_as_uint(v), CTRL, 0xf, 0xf, true)); }
DEV float sum32(float v) {
  v = dpp_add<0xB1>(v); v = dpp_add<0x4E>(v); v = dpp_add<0x141>(v); v = dpp_add<0x140>(v);
  unsigned u = __float_as_uint(v);
  auto a = __builtin_amdgcn_permlane16_swap(u, u, false, false);
  return __uint_as_float(a[0]) + __uint_as_float(a[1]);
}
DEV float sum8(float v) { v = dpp_add<0xB1>(v); v = dpp_add<0x4E>(v); return dpp_add<0x141>(v); }

DEV void poll_flags(const char* ws, unsigned off, u64 tag) {
  GAS u64* f = (GAS u64*)(ws + off);
#pragma unroll 1
  for (unsigned spins = 0; spins < (1u << 16); ++spins) {
    u64 v = __hip_atomic_load(f, RLX_AGENT);
    if (__all(v == tag)) break;
    __builtin_amdgcn_s_sleep(1);
  }
}
DEV void set_flag(char* ws, unsigned off, u64 tag) { __hip_atomic_store((GAS u64*)(ws + off), tag, RLX_AGENT); }

DEV void reduce_wk(LAS char* redb, int wm, int wk, int lane, f32x4 (&acc)[5]) {
  LAS f32x4* red = (LAS f32x4*)redb + wm * 5 * 64 + lane;
  if (wk == 0) { red[2 * 64] = acc[2]; red[3 * 64] = acc[3]; }
  else { red[0] = acc[0]; red[64] = acc[1]; red[4 * 64] = acc[4]; }
  BAR();
  if (wk == 0) { acc[0] += red[0]; acc[1] += red[64]; acc[2] = acc[4] + red[4 * 64]; }
  else { acc[0] = acc[2] + red[2 * 64]; acc[1] = acc[3] + red[3 * 64]; acc[2] = f32x4{0, 0, 0, 0}; }
}
#define MFMA_FENCE5(a) asm volatile("s_nop 7\n\ts_nop 1" : "+v"(a[0]), "+v"(a[1]), "+v"(a[2]), "+v"(a[3]), "+v"(a[4]))

template <int FRAG, int POS> DEV void bload(const char* sbw, unsigned l16, unsigned lw) {
  constexpr int sl = POS % RING;
  if constexpr (sl + 7 < RING) glds(sbw + (size_t)FRAG * 1024, l16, lw + sl * 1024u);
  else { unsigned d = lw + sl * 1024u; if (d >= RING * 1024u) d -= RING * 1024u; glds(sbw + (size_t)FRAG * 1024, l16, d); }
}

__global__ __launch_bounds__(512, 2) void mega(Params p) {
  extern __shared__ __attribute__((aligned(16))) char smem[];
  LAS char* L = (LAS char*)smem;
  const int b = blockIdx.x, tid = threadIdx.x, lane = tid & 63;
  const int w = __builtin_amdgcn_readfirstlane(tid >> 6);
  const int xcd = b & 7, j = b >> 3, grp = xcd >> 1, half = xcd & 1;
  const int rt128 = 4 * grp + (j & 3), sidx = j >> 2, cg = 8 * half + sidx;
  const int rt64 = 8 * grp + (j & 7), ct = 4 * half + (j >> 3);
  const int wm = w & 3, wk = w >> 2, r = lane & 15, g = lane >> 4;
  const unsigned l16 = lane * 16, lw = w * 1024;
  const __amdgpu_buffer_rsrc_t rs = __builtin_amdgcn_make_buffer_rsrc((void*)p.ws, 0, WS_END, 0x00020000);
  const v4u rq = {(unsigned)(size_t)p.ws, (unsigned)((size_t)p.ws >> 32) & 0xffffu, WS_END, 0x00020000u};
  const char* sb2 = (const char*)p.w2 + ct * 160;
  unsigned v2[5];
  auto w2init = [&]() {
#pragma unroll
    for (int k = 0; k < 5; ++k) { const unsigned u = (8 * k + w) * 64 + lane, row = (u * 6554) >> 16; v2[k] = row * 1120 + u * 16; PIN(v2[k]); }
  };
  auto w2load = [&](auto N) {
    constexpr int n = N;
    if constexpr (n < 12) glds(sb2 + (size_t)(n / 5) * 256 * 1280, v2[n % 5], lw + n * 8192);
    else glds(sb2 + (size_t)(w < 4 ? 512 : 256) * 1280, v2[2], lw + n * 8192);
  };
  v4u qp[6];
  u64 tag; unsigned tag32;
  v2u ep;
  STAMP(0);
  auto make_tag = [&]() {
    unsigned e0, e1;
    asm volatile("v_readfirstlane_b32 %0, %2\n\tv_readfirstlane_b32 %1, %3\n\ts_nop 3" : "=s"(e0), "=s"(e1) : "v"(ep[0]), "v"(ep[1]));
    const u64 e = ((u64)e1 << 32) | e0;
    tag = e * 6364136223846793005ull + 1442695040888963407ull; tag32 = (unsigned)(tag >> 32);
  };
  const int role = sidx < 5 ? 0 : cg != 15 ? 1 : 2;
  unsigned fhoff;
  if (__builtin_expect(role != 2, 1)) {
    const bool isq = role == 0;
    const int wm2 = w & 1, wn = w >> 1;
    const int col = isq ? 5 * half + sidx : 3 * half + sidx - 5;
    const int pitch = isq ? 3840 : 1280;
    const char* sX[5];
    sX[0] = (const char*)p.x + (size_t)rt128 * 128 * 1280; sX[1] = sX[0] + 64 * 1280;
    sX[2] = (const char*)(isq ? p.wqkv : p.w1) + col * (isq ? 384 : 256); sX[3] = sX[2] + 128; sX[4] = sX[2] + 256;
    int kc = sidx + ((5 * (rt128 & 3) + 1) >> 1); if (kc >= 10) kc -= 10;
    unsigned va, vb;
    {
      const int srow = lane >> 3, ss = lane & 7, r0 = 8 * w + srow;
      va = r0 * 1280 + ((ss ^ ((r0 >> 1) & 7)) << 4) + kc * 128;
      const int swz = ((r0 >> 1) & 1) | (((r0 >> 3) & 1) << 1);
      vb = (r0 + kc * 64) * pitch + (((((ss >> 1) ^ swz) << 1) | (ss & 1)) << 4);
    }
    auto advance = [&]() {
      va += kc == 9 ? -9 * 128 : 128; vb += (kc == 9 ? -9 * 64 : 64) * pitch;
      kc = kc == 9 ? 0 : kc + 1;
    };
    gload8<0>(ep, p.ws + WS_EP, 0u);
    v4u sx[3], sg[3];
    if (isq) {
      const unsigned vg = (lane & 31) * 16, vs = ((col & 7) * 16 + 2 * w + (lane >> 5)) * 1280 + vg;
      gload16<0>(sx[0], sX[0], vs); gload16<512>(sx[1], sX[0], vs); gload16<1024>(sx[2], sX[0], vs - (lane & 16) * 16);
      gload16<0>(sg[0], p.gw, vg); gload16<512>(sg[1], p.gw, vg); gload16<1024>(sg[2], p.gw, (unsigned)(r * 16));
    }
    sfor<2>([&](auto C) {
      constexpr int c = C; constexpr unsigned sl = ((c + 2) % 3) * 40960;
      sfor<4>([&](auto K) { constexpr int k = K; glds(sX[k], k < 2 ? va : vb, lw + sl + k * 8192); });
      if (isq) glds(sX[4], vb, lw + sl + 4 * 8192);
      advance();
    });
    {
      const int own = rt64 >> 1, nb = (own + ((rt64 & 1) ? 1 : 15)) & 15;
      const int li = lane < 10 ? lane : 0;
      fhoff = WS_FH + (li < 5 ? own * 8 + li : nb * 8 + li - 5) * 128;
    }
    auto s1run = [&](auto NT_) {
      constexpr int NT = NT_, NG = 2 + NT; constexpr bool ISQ = NT == 3;
      unsigned mslot = 40960 + lw;
      auto issue = [&](auto K) {
        constexpr int k = K;
        if constexpr (k >= 100) w2load(IC<k - 100>{});
        else {
          glds(sX[k], k < 2 ? va : vb, mslot + k * 8192);
          if constexpr (k == NG - 1) { advance(); mslot += 40960; if (mslot >= 122880) mslot -= 122880; }
        }
      };
      sfor<NG>(issue);
      w2init();
      unsigned xo[2], wo_[NT];
      xo[0] = 81920 + wm2 * 8192 + r * 128 + ((g ^ (r >> 1)) << 4);
      xo[1] = xo[0] ^ 64;
      {
        const int q = (lane >> 2) & 3, pp = lane & 3, swz = ((q >> 1) & 1) | ((g & 1) << 1);
#pragma unroll
        for (int t = 0; t < NT; ++t) { const int U = NT * wn + t; wo_[t] = 81920 + 16384 + (U >> 2) * 8192 + (8 * g + q) * 128 + (((U & 3) ^ swz) << 5) + pp * 8; PIN(wo_[t]); }
      }
      PIN(xo[0]); PIN(xo[1]); PIN(va); PIN(vb); PIN(fhoff);
      STAMP(30);
      SB();
      if constexpr (ISQ) {
        WAITV(15);
        make_tag();
        PIN(sx[0]); PIN(sx[1]); PIN(sx[2]); PIN(sg[0]); PIN(sg[1]); PIN(sg[2]);
        float sm = 0, sq = 0, gt = 0, sm2 = 0, sq2 = 0, gt2 = 0;
#pragma unroll
        for (int e = 0; e < 4; ++e) {
          sm = dot2(sx[0][e], 0x3F803F80u, sm); sq = dot2(sx[0][e], sx[0][e], sq); gt = dot2(sx[0][e], sg[0][e], gt);
          sm2 = dot2(sx[2][e], 0x3F803F80u, sm2); sq2 = dot2(sx[2][e], sx[2][e], sq2); gt2 = dot2(sx[2][e], sg[2][e], gt2);
          sm = dot2(sx[1][e], 0x3F803F80u, sm); sq = dot2(sx[1][e], sx[1][e], sq); gt = dot2(sx[1][e], sg[1][e], gt);
        }
        sm = sum32(__builtin_fmaf(sm2, 0.5f, sm)); sq = sum32(__builtin_fmaf(sq2, 0.5f, sq)); gt = sum32(__builtin_fmaf(gt2, 0.5f, gt));
        const float mean = sm * (1.0f / D);
        const float rstd = __builtin_amdgcn_rsqf(fmaxf(sq * (1.0f / D) - mean * mean, 0.0f) + 1e-5f);
        const float gate = __builtin_amdgcn_rcpf(1.0f + __expf(-gt));
        BSTORE16((v4u{__float_as_uint((lane & 1) ? gate : mean), tag32, __float_as_uint(rstd), tag32}), ((lane & 30) == 0 && col < 8) ? (lane >> 5) * 16 + (lane & 1) * (WS_GT - WS_RS) : OOB,
                 WS_RS + (rt128 * 128 + (col & 7) * 16 + 2 * w) * 16);
        SB();
        WAITV(11);
      } else WAITV(2 * NG);
      RAWBAR();
      STAMP(1);
      f32x4 acc[NT][4];
      bf16x8 xf0[4], xf1[4], wa0, wa1, wa2, wb0, wb1, wb2;
      int dnext = -2 * 40960;
#define MM_(t, m) do { if constexpr (P == 0) { if constexpr (t == 0) MMW(fi, acc[0][m], wa0, 100, 103, xf0[m]); else if constexpr (t == 1) MMW(fi, acc[1][m], wa1, 104, 107, xf0[m]); else MMW(fi, acc[NT - 1][m], wa2, 108, 111, xf0[m]); } \
    else { if constexpr (t == 0) MMW(fi, acc[0][m], wb0, 112, 115, xf1[m]); else if constexpr (t == 1) MMW(fi, acc[1][m], wb1, 116, 119, xf1[m]); else MMW(fi, acc[NT - 1][m], wb2, 120, 123, xf1[m]); } } while (0)
#define RW_(P, t) do { if constexpr (P == 1) { if constexpr (t == 0) TRW(wa0, 100, 101, 102, 103, wo_[0], 0); else if constexpr (t == 1) TRW(wa1, 104, 105, 106, 107, wo_[1], 0); else TRW(wa2, 108, 109, 110, 111, wo_[NT - 1], 0); } \
    else { if constexpr (t == 0) TRW(wb0, 112, 113, 114, 115, wo_[0], 4096); else if constexpr (t == 1) TRW(wb1, 116, 117, 118, 119, wo_[1], 4096); else TRW(wb2, 120, 121, 122, 123, wo_[NT - 1], 4096); } } while (0)
#define RX_(P, m) do { if constexpr (P == 1) ADSR(xf0[m], xo[0], (m) * 2048); else ADSR(xf1[m], xo[1], (m) * 2048); } while (0)
      auto kstep = [&](auto P_, auto G0, auto GN, auto RD, auto UPD, auto FIRST) {
        constexpr int P = P_, g0 = G0, gn = GN; constexpr bool rd = RD, upd = UPD, fi = FIRST;
        MM_(0, 0); if constexpr (rd) RW_(P, 0);
        MM_(0, 1); if constexpr (rd) RX_(P, 0);
        MM_(0, 2); if constexpr (rd) RX_(P, 1);
        MM_(0, 3); if constexpr (rd) { RX_(P, 2); if constexpr (NT == 2) RX_(P, 3); }
        MM_(1, 0); if constexpr (rd) { if constexpr (NT == 3) RX_(P, 3); else RW_(P, 1); }
        if constexpr (NT == 2 && gn > 0) issue(IC<g0>{});
        MM_(1, 1); if constexpr (rd && NT == 3) RW_(P, 1);
        MM_(1, 2); if constexpr (rd && NT == 3) RW_(P, 2);
        if constexpr (NT == 2 && gn > 1) issue(IC<g0 + 1>{});
        if constexpr (NT == 3 && gn > 0) issue(IC<g0>{});
        MM_(1, 3);
        if constexpr (NT == 3) {
          MM_(2, 0);
          if constexpr (gn > 1) issue(IC<g0 + 1>{});
          MM_(2, 1); MM_(2, 2);
          if constexpr (gn > 2) issue(IC<g0 + 2>{});
          if constexpr (upd) { wo_[0] += dnext; wo_[1] += dnext; wo_[NT - 1] += dnext; xo[0] += dnext; xo[1] += dnext; }
          SB();
          MM_(2, 3);
        } else {
          if constexpr (upd) { wo_[0] += dnext; wo_[1] += dnext; xo[0] += dnext; xo[1] += dnext; }
        }
        SB();
      };
      using BT = std::true_type; using BF = std::false_type;
      RW_(1, 0); RX_(1, 0); RX_(1, 1); RX_(1, 2); RX_(1, 3); RW_(1, 1); if constexpr (NT == 3) RW_(1, 2);
      WAITL(0);
      SB();
      kstep(IC<0>{}, IC<0>{}, IC<0>{}, BT{}, BT{}, BT{});
      if constexpr (ISQ) {
        auto iter = [&](auto G0, auto LAST) {
          constexpr int g0 = G0;
          BAR();
          kstep(IC<1>{}, IC<g0>{}, IC<3>{}, BT{}, BF{}, BF{});
          WAITL(0);
          kstep(IC<0>{}, IC<g0 + 3>{}, IC<2>{}, BT{}, std::integral_constant<bool, !LAST>{}, BF{});
        };
        dnext = 40960;
        WAITV(6); iter(IC<0>{}, BF{});
        WAITV(6); iter(IC<0>{}, BF{});
#pragma unroll 1
        for (int ch = 2; ch < 7; ++ch) { dnext = (ch == 2 || ch == 5) ? -2 * 40960 : 40960; WAITV(5); iter(IC<0>{}, BF{}); }
        WAITV(5); iter(IC<100>{}, BF{});
        WAITV(5); iter(IC<105>{}, BT{});
        BAR();
        kstep(IC<1>{}, IC<110>{}, IC<3>{}, BF{}, BF{}, BF{});
        asm volatile("s_nop 7\n\ts_nop 1" : "+v"(acc[2][0]), "+v"(acc[2][1]), "+v"(acc[2][2]), "+v"(acc[2][3]));
        STAMP(2);
#pragma unroll
        for (int t = 0; t < 3; ++t)
#pragma unroll
          for (int mp = 0; mp < 2; ++mp) {
            PIN(acc[t][2 * mp]); PIN(acc[t][2 * mp + 1]);
            f32x4 a = acc[t][2 * mp], bb = acc[t][2 * mp + 1];
            qp[2 * t + mp] = v4u{pk2(a[0], a[1]), pk2(a[2], a[3]), pk2(bb[0], bb[1]), pk2(bb[2], bb[3])};
          }
        STAMP(3);
      } else {
        unsigned rslot = 0;
#pragma unroll 1
        for (int ch = 0; ch < 7; ++ch) {
          dnext = rslot == 2 ? -2 * 40960 : 40960;
          rslot = rslot == 2 ? 0 : rslot + 1;
          WAITV(NG);
          BAR();
          kstep(IC<1>{}, IC<0>{}, IC<NT>{}, BT{}, BF{}, BF{});
          WAITL(0);
          kstep(IC<0>{}, IC<NT>{}, IC<2>{}, BT{}, BT{}, BF{});
        }
        make_tag();
        dnext = 40960;
        WAITV(NG);
        BAR();
        v4u st[4], cs[4];
        const unsigned sto = WS_RS + (rt128 * 128 + wm2 * 64) * 16, cso = WS_CS + (col * 128 + wn * 32) * 8;
        sfor<4>([&](auto M) { constexpr int m = M; gload16<m * 256>(st[m], p.ws + sto, (unsigned)(r * 16)); });
        kstep(IC<1>{}, IC<0>{}, IC<0>{}, BT{}, BF{}, BF{});
        sfor<4>([&](auto M) { constexpr int m = M; gload16<(m >> 1) * 128 + (m & 1) * 16>(cs[m], p.ws + cso, (unsigned)(g * 32)); });
        WAITL(0);
        kstep(IC<0>{}, IC<100>{}, IC<1>{}, BT{}, BT{}, BF{});
        WAITV(9);
        BAR();
        kstep(IC<1>{}, IC<101>{}, IC<2>{}, BT{}, BF{}, BF{});
        WAITL(0);
        kstep(IC<0>{}, IC<103>{}, IC<1>{}, BT{}, BF{}, BF{});
        BAR();
        kstep(IC<1>{}, IC<104>{}, IC<2>{}, BF{}, BF{}, BF{});
        STAMP(2);
        asm volatile("s_waitcnt vmcnt(6)\n\ts_nop 7" : "+v"(st[0]), "+v"(st[1]), "+v"(st[2]), "+v"(st[3]), "+v"(cs[0]), "+v"(cs[1]), "+v"(cs[2]), "+v"(cs[3])::"memory");
#pragma unroll 1
        for (unsigned spins = 0; spins < (1u << 16); ++spins) {
          unsigned bad = 0;
#pragma unroll
          for (int m = 0; m < 4; ++m) bad |= (st[m][1] ^ tag32) | (st[m][3] ^ tag32) | (cs[m][1] ^ tag32) | (cs[m][3] ^ tag32);
          if (__all(bad == 0)) break;
#pragma unroll
          for (int m = 0; m < 4; ++m) {
            st[m] = __builtin_amdgcn_raw_buffer_load_b128(rs, r * 16 + m * 256, sto, 16);
            cs[m] = __builtin_amdgcn_raw_buffer_load_b128(rs, g * 32 + (m >> 1) * 128 + (m & 1) * 16, cso, 16);
          }
        }
        STAMP(31);
        const int hp = 4 * col + wn;
        const int gq = (hp * 13) >> 6;
        const int delta = gq == 0 ? 1 : gq == 1 ? -1 : gq == 2 ? 2 : -2;
        sfor<4>([&](auto M_) {
          constexpr int m = M_;
          const float mean = __uint_as_float(st[m][0]), rstd = __uint_as_float(st[m][2]);
          const float k1 = GC1 * rstd, k2 = GC2 * rstd * rstd * rstd, sd = __builtin_amdgcn_rcpf(rstd);
          int drow = rt128 * 128 + wm2 * 64 + m * 16 + r + delta;
          const bool wrap = (unsigned)drow >= (unsigned)S;
          drow &= (S - 1);
          const unsigned fb = (drow >> 4) * 20 * 1024 + (g * 16 + (drow & 15)) * 16;
          v4u o;
#pragma unroll
          for (int t = 0; t < 2; ++t) {
            pin(acc[t][m]);
#pragma unroll
            for (int h = 0; h < 2; ++h) {
              float h0 = gelu_s(__builtin_fmaf(-mean, __uint_as_float(cs[2 * t + h][0]), acc[t][m][2 * h]), k1, k2, sd);
              float h1 = gelu_s(__builtin_fmaf(-mean, __uint_as_float(cs[2 * t + h][2]), acc[t][m][2 * h + 1]), k1, k2, sd);
              o[2 * t + h] = wrap ? 0u : pk2(h0, h1);
            }
          }
          BSTORE16(o, fb, WS_HS + hp * 1024);
          SB();
        });
        STAMP(29);
        WAITV(0);
        RAWBAR();
        if (tid == 0) set_flag(p.ws, WS_FH + (rt128 * 8 + col) * 128, tag);
        STAMP(3);
        sfor<7>([&](auto I) { w2load(IC<6 + I>{}); });
      }
    };
    if (!isq) s1run(IC<2>{}); else s1run(IC<3>{});
  } else {
    const int z = rt128;
    {
      const int own = rt64 >> 1, nb = (own + ((rt64 & 1) ? 1 : 15)) & 15;
      const int li = lane < 10 ? lane : 0;
      fhoff = WS_FH + (li < 5 ? own * 8 + li : nb * 8 + li - 5) * 128;
    }
    gload8<0>(ep, p.ws + WS_EP, 0u);
    v4u selu;
#pragma unroll
    for (int e = 0; e < 4; ++e) selu[e] = r == 2 * e ? 0x3F80u : r == 2 * e + 1 ? 0x3F800000u : 0u;
    const bf16x8 sel = __builtin_bit_cast(bf16x8, selu);
    w2init();
    v4u cr[3][10];
    auto zload = [&](auto K) {
      constexpr int k = K;
      const int task = z + 16 * k < 37 ? z + 16 * k : z;
      const int tp = task < 10 ? 1280 : 3456;
      const char* src = (task < 10 ? (const char*)p.w1 + task * 128 : (const char*)p.w3 + (task - 10) * 128) + (size_t)w * 80 * tp;
      const unsigned vx = (lane >> 3) * tp + (lane & 7) * 16;
      if (k < 2 || z < 5) sfor<10>([&](auto I) { constexpr int i = I; gload16<0>(cr[k][i], src + (size_t)i * 8 * tp, vx); });
      else sfor<10>([&](auto I) {
        constexpr int i = I; gload16<0>(cr[k][i], (const char*)p.w2 + (size_t)(z - 5) * 73728 + i * 8192, l16 + lw); });
    };
    zload(IC<0>{}); zload(IC<1>{});
    sfor<3>([&](auto K) {
      constexpr int k = K;
      LAS float* cp = (LAS float*)(L + k * 4096);
      if constexpr (k == 1) zload(IC<2>{});
      WAITV(k < 2 ? 10 : 0);
      if constexpr (k == 0) make_tag();
      f32x4 c = {0, 0, 0, 0};
#pragma unroll
      for (int i = 0; i < 10; ++i) { pin(cr[k][i]); c = mfma(sel, __builtin_bit_cast(bf16x8, cr[k][i]), c); }
      if (g < 2) *(LAS f32x4*)(cp + (w * 2 + (r >> 3)) * 64 + (r & 7) * 8 + g * 4) = c;
      BAR();
      if (w == 1 + k) {
        f32x2 s = {0, 0};
#pragma unroll
        for (int i = 0; i < 16; ++i) s += *(LAS f32x2*)(cp + i * 64 + (lane & 31) * 2);
        BSTORE16((v4u{__float_as_uint(s[0]), tag32, __float_as_uint(s[1]), tag32}), (lane < 32 && z + 16 * k < 37) ? l16 : OOB, WS_CS + (z + 16 * k) * 512);
      }
      STAMP(1 + k);
    });
    BAR();
    sfor<13>(w2load);
  }
  v4u xr; v2u xs;
  const unsigned orow = (unsigned)(rt64 * 64 + wm * 16 + r) * 1280 + ct * 160;
  const unsigned opair = orow + wk * 64 + (g & 1) * 32 + (g >> 1) * 16;
  const int q4 = (lane >> 2) & 3, pp = lane & 3;
  unsigned v3[7];
  const int cmax3 = cg < 12 ? 13 : 11;
#pragma unroll
  for (int k = 0; k < 7; ++k) {
    const unsigned u = (8 * k + w) * 64 + lane, row = (u * 4682) >> 16; unsigned ch = u - row * 14; if (ch > cmax3) ch = cmax3;
    v3[k] = (row + (row >> 6) * 16) * 3456 + ch * 16; PIN(v3[k]);
  }
  const char* sb3 = (const char*)p.w3 + (6 * cg + (cg < 12 ? cg : 12)) * 32;
  unsigned v3c[4];
#pragma unroll
  for (int k = 0; k < 4; ++k) {
    const unsigned u = (8 * k + (k == 3 ? (w & 3) : w)) * 64 + lane, row = (u * 4682) >> 16; unsigned ch = u - row * 14; if (ch > cmax3) ch = cmax3;
    v3c[k] = (160 * (row >> 5) + 64 + 80 * ((row >> 3) & 1) + 8 * ((row >> 2) & 1) + 4 * ((row >> 4) & 1) + (row & 3)) * 3456 + ch * 16; PIN(v3c[k]);
  }
  v4u w3s[7];
  auto w3load = [&](auto N) {
    constexpr int n = N;
    const unsigned dst = n < 4 ? lw + (120 + 8 * n) * 1024 : n > 4 ? lw + (8 * n - 35) * 1024 : (w < 3 ? lw + 152 * 1024 : lw - 3 * 1024);
    if constexpr (n < 11) glds(sb3 + (size_t)(n / 7) * 320 * 3456, v3[n % 7], dst);
    else if constexpr (n < 14) gload16<0>(w3s[n - 11], sb3 + (size_t)320 * 3456, v3[n % 7]);
    else gload16<0>(w3s[n - 11], sb3, v3c[n - 14]);
  };
  const char* sb4 = p.ws + WS_W4 + ct * 370 * 1024 + lw;
  sfor<4>(w3load);
  if (w == 7) poll_flags(p.ws, fhoff, tag);
  WAITV(4);
  gload16<0>(xr, p.x, opair);
  gload8<128>(xs, p.x, orow + g * 8);
  RAWBAR();
  STAMP(4);
  f32x4 acc[5];
  unsigned x1p[3][2];
  v2u jf[4];
  {
    bf16x8 af[10];
    const char* ap = p.ws + WS_HS + (size_t)((rt64 * 4 + wm) * 20 + wk) * 1024;
    sfor<10>([&](auto I) { constexpr int i = I; gload16<(i & 1) * 2048>(af[i], ap + (i >> 1) * 4096, l16); });
    {
      const int e = (lane >= 10) + (lane >= 20) + (lane >= 30);
      const unsigned vo = e * 1120 + l16;
      const int k0 = xcd * 4 + (j >> 3);
      if (lane < 40) {
#pragma unroll
        for (int i = 0; i < 2; ++i) {
          int ks = k0 + 32 * i; if (ks >= 54) ks = 0;
          const int c3 = (ks * 43) >> 7;
          int R = 16 * (6 * c3 + (c3 < 12 ? c3 : 12)) + 32 * (ks - 3 * c3) + (w & 1) * 16 + (w >> 1) * 4;
          if (ks >= 48) R = 224 * (ks - 48) + 96 + (w & 3) * 4 + (w >> 2) * 112;
          glds((const char*)p.w4 + (size_t)R * 1280 + (j & 7) * 160, vo, L_JOB + i * 5120 + w * 640);
        }
        const int ks = k0 < 20 ? k0 : 0;
        const int R = ks < 16 ? 40 * ks + (w & 1) * 20 + (w >> 1) * 4 : 160 * (ks - 16) + 16 + 20 * w;
        glds((const char*)p.wo + (size_t)R * 1280 + (j & 7) * 160, vo, L_JOB + 10240 + w * 640);
      }
    }
    bf16x8 bfr[5];
    const unsigned b0 = wk * 5120 + (4 * g + q4) * 160 + pp * 8, b1 = b0 + 51200;
    sfor<5>([&](auto T) { constexpr int t = T; trb<t, t * 32, t * 32 + 2560>(bfr[t], b0); });
    sfor<10>([&](auto I) {
      constexpr int i = I;
      WAITV_R(9 - i + 3 + (i > 2 ? i - 2 : 0), af[i]);
      if constexpr (i == 5) RAWBAR();
      sfor<5>([&](auto T) {
        constexpr int t = T;
        if constexpr (i < 9) WAITL(8); else WAITL(8 - 2 * t);
        mmb<t, i == 0>(acc[t], bfr[t], af[i]);
        if constexpr (i < 9) { constexpr int o = ((i + 1) % 5) * 10240 + t * 32; trb<t, o, o + 2560>(bfr[t], i + 1 < 5 ? b0 : b1); }
        if constexpr (t == 2 && i >= 2 && i < 9) w3load(IC<(i < 9 ? 9 + i : 11)>{});
      });
    });
    WAITV(7);
    MFMA_FENCE5(acc);
    STAMP(5);
    reduce_wk(L, wm, wk, lane, acc);
    {
      const int f1 = w < 7 ? 8 + w : 14;
      const unsigned ja = L_JOB + g * 1280 + q4 * 160 + pp * 8;
      const unsigned j0 = ja + (w < 5 ? w * 32 : 5120 + (w - 5) * 32), j1 = ja + (f1 < 10 ? 5120 + (f1 - 5) * 32 : 10240 + (f1 - 10) * 32);
      SB();
      ADSTR(jf[0], j0, 0); ADSTR(jf[1], j0, 640); ADSTR(jf[2], j1, 0); ADSTR(jf[3], j1, 640);
    }
    PIN(xr); PIN(xs);
    const v2u xa = sw16(xr[0], xr[2]), xb = sw16(xr[1], xr[3]);
    float sum = 0, sq = 0;
#pragma unroll
    for (int i = 0; i < 3; ++i) {
      const unsigned x0 = i < 2 ? xa[i] : xs[0], x1 = i < 2 ? xb[i] : xs[1];
      x1p[i][0] = pk2(bflo(x0) + acc[i][0], bfhi(x0) + acc[i][1]); x1p[i][1] = pk2(bflo(x1) + acc[i][2], bfhi(x1) + acc[i][3]);
      if (i < 2 || wk == 0) {
        sum = dot2(x1p[i][0], 0x3F803F80u, sum); sum = dot2(x1p[i][1], 0x3F803F80u, sum);
        sq = dot2(x1p[i][0], x1p[i][0], sq); sq = dot2(x1p[i][1], x1p[i][1], sq);
      }
    }
    const unsigned so = WS_X1 + (rt64 * 4 + wm) * 20 * 1024;
    BSTORE16((v4u{x1p[0][0], x1p[0][1], x1p[1][0], x1p[1][1]}), l16, so + (2 * ct + wk) * 1024);
    const v2u s0 = sw16(x1p[2][0], x1p[2][0]), s1 = sw16(x1p[2][1], x1p[2][1]);
    BSTORE16((v4u{s0[0], s1[0], s0[1], s1[1]}), (wk == 0 && !(g & 1)) ? (g >> 1) * 256 + r * 16 : OOB, so + 16 * 1024 + ct * 512);
    sum = xsum4(sum); sq = xsum4(sq);
    __builtin_amdgcn_raw_buffer_store_b64(__builtin_bit_cast(v2u, f32x2{sum, sq}), rs, g == 0 ? r * 8 : OOB, WS_ST + ((rt64 * 4 + wm) * 16 + ct * 2 + wk) * 128, 16);
  }
  {
    asm volatile("s_waitcnt lgkmcnt(0)" : "+v"(jf[0]), "+v"(jf[1]), "+v"(jf[2]), "+v"(jf[3])::"memory");
    const int k0 = xcd * 4 + (j >> 3), f1 = w < 7 ? 8 + w : 14;
    const unsigned d0 = WS_W4 + ((j & 7) * 74 + k0) * 5120;
    BSTORE16((v4u{jf[0][0], jf[0][1], jf[1][0], jf[1][1]}), (w < 5 || k0 < 22) ? l16 : OOB, d0 + (w < 5 ? w * 1024 : 32 * 5120 + (w - 5) * 1024));
    BSTORE16((v4u{jf[2][0], jf[2][1], jf[3][0], jf[3][1]}), (f1 < 10 ? k0 < 22 : (k0 < 20 && w < 7)) ? l16 : OOB, d0 + (f1 < 10 ? 32 * 5120 + (f1 - 5) * 1024 : 54 * 5120 + (f1 - 10) * 1024));
    WAITV(0);
    RAWBAR();
    if (tid < 2) set_flag(p.ws, tid == 0 ? WS_F2 + (rt64 * 8 + ct) * 128 : WS_FW + b * 8, tag);
    STAMP(6);
  }
  sfor<7>([&](auto I) { w3load(IC<I + 4>{}); });
  sfor<7>([&](auto I) { constexpr int n = 11 + I; dsw16<(n - 11) * 8192>(l16 + 53 * 1024 + (n == 17 ? (w & 3) * 1024 : lw), w3s[I]); });
  {
    if (w == 6) {
      const unsigned co = WS_CS + (640 + (6 * cg + (cg < 12 ? cg : 12)) * 16) * 8, cv_ = (lane < (cg < 12 ? 56 : 48) ? lane : 0) * 16;
      v4u c;
#pragma unroll 1
      for (unsigned spins = 0; spins < (1u << 16); ++spins) {
        c = __builtin_amdgcn_raw_buffer_load_b128(rs, cv_, co, 16);
        if (__all(c[1] == tag32 && c[3] == tag32)) break;
      }
      *(LAS v2u*)(L + L_CS3 + lane * 8) = v2u{c[0], c[2]};
    }
    if (w == 7) poll_flags(p.ws, WS_F2 + (rt128 * 16 + r) * 128, tag);
    WAITV(0);
    BAR();
    STAMP(8);
    const int rt = rt128 * 8 + w;
    constexpr int PD = 10;
    bf16x8 af[20];
    const char* ap = p.ws + WS_X1 + (size_t)rt * 20 * 1024;
    sfor<PD>([&](auto I) { constexpr int i = I; gload16<(i & 3) * 1024>(af[i], ap + (i >> 2) * 4096, l16); });
    f32x4 acc3[7]; f32x2 stp[4];
    bf16x8 bfr[7];
    const unsigned c1 = (4 * g + q4) * 224 + pp * 8, c0 = c1 + 122880, c2 = c1 + 57344;
    sfor<7>([&](auto T) { constexpr int t = T; trb<t, t * 32, t * 32 + 3584>(bfr[t], c0); });
    u64 fwv, fqv;
    const unsigned fwoff = WS_FW + (w * 32 + (lane & 31)) * 8;
    unsigned fqoff;
    {
      const int own = rt64 >> 1, pv = ((rt64 & 1) || own == 0) ? own : own - 1;
      fqoff = WS_FQ + (lane < 10 ? own * 128 + lane * 8 : lane < 20 ? pv * 128 + (lane - 10) * 8 : own * 128);
    }
    const unsigned qso = WS_QKV + ((rt128 * 4 + (w & 1) * 2) * 120 + (5 * half + sidx) * 12 + (w >> 1) * 3) * 1024;
    int art = 2 * rt64 + (w < 2 ? w : w < 5 ? w - 3 : w - 6); if (art < 0) art = 0;
    const char* asb = p.ws + WS_QKV + (size_t)(art * 120 + (w < 2 ? 0 : w < 5 ? 40 : 80) + 5 * ct) * 1024;
    sfor<20>([&](auto I) {
      constexpr int ks = I;
      constexpr auto nA = [](int s) { return s <= 17 ? 1 : 0; };
      constexpr auto nB = [](int s) { return (s == 7 || s >= 9) ? 1 : 0; };
      constexpr int cnt = [&]() {
        int n = 0;
        if (ks < PD) { n = PD - 1 - ks; for (int s = 0; s < ks; ++s) n += nA(s) + nB(s); }
        else { int s0 = ks - PD; n = nB(s0); for (int s = s0 + 1; s < ks; ++s) n += nA(s) + nB(s); }
        return n; }();
      if constexpr (ks == 1 || ks == 5) STAMP(16 + ks / 4);
      if constexpr (ks == 9) {
        STAMP(18);
        WAITV_R(1, fwv);
        if (!__all(fwv == tag)) poll_flags(p.ws, fwoff, tag);
      }
      WAITV_R(cnt, af[ks]);
      if constexpr (ks == 9 || ks == 12 || ks == 15) RAWBAR();
      if constexpr (ks == 9) STAMP(19);
      if constexpr (ks == 15) STAMP(21);
      if constexpr (ks == 9) { if (tid == 0 && role == 0) set_flag(p.ws, WS_FQ + rt128 * 128 + (5 * half + sidx) * 8, tag); }
      sfor<7>([&](auto T) {
        constexpr int t = T;
        if constexpr (ks < 19) WAITL(12); else WAITL(12 - 2 * t);
        mmb<t, ks == 0>(acc3[t], bfr[t], af[ks]);
        if constexpr (ks < 19) { constexpr int k1 = ks + 1, o = (k1 < 5 ? k1 : k1 < 13 ? k1 - 5 : k1 - 13) * 7168 + t * 32; trb<t, o, o + 3584>(bfr[t], k1 < 5 ? c0 : k1 < 13 ? c1 : c2); }
        if constexpr (t == 1) {
          if constexpr (ks < 10) gload16<((ks + PD) & 3) * 1024>(af[ks + PD], ap + ((ks + PD) >> 2) * 4096, l16);
          else if constexpr (ks < 14) gload8<(ks - 10) * 128>(stp[ks - 10], p.ws + WS_ST + (size_t)rt * 2048, (unsigned)(g * 512 + r * 8));
          else if constexpr (ks < 17) { constexpr int f = 40 + 16 * (ks - 14); bload<f, B0 + f>(sb4, l16, lw); }
          else if constexpr (ks == 17) gload8<0>(fqv, p.ws, fqoff);
        }
        if constexpr (t == 4) {
          if constexpr (ks == 7) gload8<0>(fwv, p.ws, fwoff);
          else if constexpr (ks >= 9) {
            constexpr int fs[11] = {0, 8, 16, 24, 32, 48, 64, 80, 88, 96, 102};
            bload<fs[ks - 9], B0 + fs[ks - 9]>(sb4, l16, lw);
          }
        }
        if constexpr (t == 6 && ks < 6) { if (role == 0) BSTORE16(qp[ks], l16, qso + (ks >> 1) * 1024 + (ks & 1) * 120 * 1024); }
      });
    });
    STAMP(20);
    WAITV_R(3, fqv);
    if (!__all(fqv == tag)) poll_flags(p.ws, fqoff, tag);
    f32x4 cv[7];
    const unsigned cva = L_CS3 + g * 16;
    sfor<7>([&](auto T) { constexpr int t = T; dsr16<t * 64>(cv[t], cva); });
    asm volatile("s_barrier\n\ts_nop 7" : "+v"(acc3[0]), "+v"(acc3[1]), "+v"(acc3[2]), "+v"(acc3[3]), "+v"(acc3[4]), "+v"(acc3[5]), "+v"(acc3[6]), "+v"(stp[0]), "+v"(stp[1]), "+v"(stp[2]), "+v"(stp[3])::"memory");
    STAMP(9);
    glds(asb, l16, L_ATT + w * 5120); glds_same_m0<1024>(asb, l16); glds_same_m0<2048>(asb, l16); glds_same_m0<3072>(asb, l16);
    glds(asb + 4096, l16, L_ATT + w * 5120 + 4096);
    const float sum = xsum4((stp[0][0] + stp[1][0]) + (stp[2][0] + stp[3][0]));
    const float sq = xsum4((stp[0][1] + stp[1][1]) + (stp[2][1] + stp[3][1]));
    const float mean = sum * (1.0f / D);
    const float var = fmaxf(sq * (1.0f / D) - mean * mean, 0.f) + 1e-5f;
    const float rstd = __builtin_amdgcn_rsqf(var), sd = var * rstd;
    const float k1 = GC1 * rstd, k2 = GC2 * rstd * rstd * rstd;
    asm volatile("s_waitcnt lgkmcnt(0)" : "+v"(cv[0]), "+v"(cv[1]), "+v"(cv[2]), "+v"(cv[3]), "+v"(cv[4]), "+v"(cv[5]), "+v"(cv[6]));
    const unsigned so = WS_U + rt * 54 * 1024;
    sfor<4>([&](auto PR) {
      constexpr int pr = PR;
      v4u o;
#pragma unroll
      for (int tt = 0; tt < (pr < 3 ? 2 : 1); ++tt) {
        const int t = 2 * pr + tt;
        float h[4];
#pragma unroll
        for (int e = 0; e < 4; ++e) h[e] = gelu_s(__builtin_fmaf(-mean, cv[t][e], acc3[t][e]), k1, k2, sd);
        o[2 * tt] = pk2(h[0], h[1]); o[2 * tt + 1] = pk2(h[2], h[3]);
      }
      if constexpr (pr < 3) BSTORE16(o, l16, so + (3 * cg + pr) * 1024);
      else {
        const v2u s0 = sw16(o[0], o[0]), s1 = sw16(o[1], o[1]);
        BSTORE16((v4u{s0[0], s1[0], s0[1], s1[1]}), (cg < 12 && !(g & 1)) ? (g >> 1) * 256 + r * 16 : OOB, so + 48 * 1024 + cg * 512);
      }
      SB();
    });
    STAMP(10);
  }
  constexpr int NSTG = 24;
  v4u stg[NSTG];
  auto stage = [&](auto Q) { constexpr int q = Q; gload16<0>(stg[q], sb4 + (size_t)(150 + 8 * q) * 1024, l16); };
  WAITV(4);
  RAWBAR();
  {
    const unsigned offc = (wm >> 1) * 5120 + (wm & 1) * 8;
    const unsigned offp = ((wm + 1) >> 1) * 5120 + ((wm + 1) & 1) * 8;
    auto qaddr = [&](int Q, int row) { return (unsigned)(L_ATT + (Q >> 2) * 1024 + ((Q & 3) * 16 + row) * 16); };
    bf16x4 ql[2], qh[2], kpl[2], kph[2], kcl[2], kch[2], vp0[2], vc0[2], vp1[2], vc1[2];
#pragma unroll
    for (int h = 0; h < 2; ++h) {
      const int hh = 2 * wk + h;
      const unsigned a0 = qaddr(5 * hh + g, r), a1 = qaddr(5 * hh + 4, r);
      const unsigned v0 = qaddr(5 * hh + pp, 4 * g + q4), v1 = qaddr(5 * hh + 4, 4 * g + q4);
      ADSR8(ql[h], a0 + offc, 0); ADSR8(qh[h], a1 + offc, 0);
      ADSR8(kcl[h], a0 + offc, 15360); ADSR8(kch[h], a1 + offc, 15360);
      ADSR8(kpl[h], a0 + offp, 10240); ADSR8(kph[h], a1 + offp, 10240);
      ADSTR(vc0[h], v0 + offc, 30720); ADSTR(vc1[h], v1 + offc, 30720);
      ADSTR(vp0[h], v0 + offp, 25600); ADSTR(vp1[h], v1 + offp, 25600);
    }
    asm volatile("s_waitcnt lgkmcnt(0)" : "+v"(ql[0]), "+v"(qh[0]), "+v"(kcl[0]), "+v"(kch[0]), "+v"(kpl[0]), "+v"(kph[0]), "+v"(vc0[0]), "+v"(vc1[0]), "+v"(vp0[0]), "+v"(vp1[0]),
                 "+v"(ql[1]), "+v"(qh[1]), "+v"(kcl[1]), "+v"(kch[1]), "+v"(kpl[1]), "+v"(kph[1]), "+v"(vc0[1]), "+v"(vc1[1]), "+v"(vp0[1]), "+v"(vp1[1])::"memory");
    const int dd = r - 4 * g;
    const bool prev_ok = (rt64 | wm) != 0;
    unsigned o0p[2][2], o1p[2][2];
#pragma unroll
    for (int h = 0; h < 2; ++h) {
      bf16x4 qhh = qh[h];
      if (g != 0) qhh = bf16x4{0, 0, 0, 0};
      const bf16x8 qf = cat(ql[h], qhh);
      const f32x4 z = {0, 0, 0, 0};
      f32x4 sp = mfma(cat(kpl[h], kph[h]), qf, z), sc = mfma(cat(kcl[h], kch[h]), qf, z);
      const float NEG = -__builtin_inff();
      float mx = NEG;
#pragma unroll
      for (int e = 0; e < 4; ++e) {
        const int dc = dd - e;
        const bool even = (dc & 1) == 0;
        const bool vc = even && dc >= 0, vp = even && dc < 0 && prev_ok;
        sp[e] = vp ? sp[e] : NEG; sc[e] = vc ? sc[e] : NEG;
        mx = fmaxf(mx, fmaxf(sp[e], sc[e]));
      }
      mx = xmax4(mx);
      const float cexp = 0.22360679774997896f * 1.4426950408889634f;
      const float mc = mx * cexp;
      float sum = 0;
#pragma unroll
      for (int e = 0; e < 4; ++e) {
        sp[e] = __builtin_amdgcn_exp2f(__builtin_fmaf(sp[e], cexp, -mc)); sc[e] = __builtin_amdgcn_exp2f(__builtin_fmaf(sc[e], cexp, -mc));
        sum += sp[e] + sc[e];
      }
      sum = xsum4(sum);
      const v4u pu = {pk2(sp[0], sp[1]), pk2(sp[2], sp[3]), pk2(sc[0], sc[1]), pk2(sc[2], sc[3])};
      const bf16x8 pf = __builtin_bit_cast(bf16x8, pu);
      const f32x4 o0 = mfma(cat(vp0[h], vc0[h]), pf, z), o1 = mfma(cat(vp1[h], vc1[h]), pf, z);
      const float inv = __builtin_amdgcn_rcpf(sum);
      o0p[h][0] = pk2(o0[0] * inv, o0[1] * inv); o0p[h][1] = pk2(o0[2] * inv, o0[3] * inv);
      o1p[h][0] = pk2(o1[0] * inv, o1[1] * inv); o1p[h][1] = pk2(o1[2] * inv, o1[3] * inv);
      if (h == 0) {
        SB();
        WAITV(0);
        RAWBAR();
        if (tid == 0) set_flag(p.ws, WS_F3 + (rt128 * 16 + cg) * 128, tag);
        STAMP(11);
        sfor<5>([&](auto I) { constexpr int f = 110 + 8 * I; bload<f, B0 + f>(sb4, l16, lw); });
        sfor<10>(stage);
        SB();
      }
    }
    const unsigned so = WS_O + (rt64 * 4 + wm) * 20 * 1024;
    BSTORE16((v4u{o0p[0][0], o0p[0][1], o0p[1][0], o0p[1][1]}), l16, so + (2 * ct + wk) * 1024);
    BSTORE16((v4u{o1p[0][0], o1p[0][1], o1p[1][0], o1p[1][1]}), g == 0 ? l16 : OOB, so + 16 * 1024 + ct * 512 + wk * 256);
  }
  STAMP(12);
  {
    if (w == 7) poll_flags(p.ws, WS_F3 + ((rt64 >> 1) * 16 + r) * 128, tag);
    sfor<14>([&](auto I) { stage(IC<10 + I>{}); });
    if (b == 0 && tid == 0) set_flag(p.ws, WS_EP, tag);
    WAITV(26);
    RAWBAR();
    STAMP(13);
    const unsigned bA0 = l16 + wk * 5120, bA1 = bA0 + 61440, bA2 = bA0 + 122880;
#define BRD(dst, s) do { if constexpr ((s) < 60) ADSR(dst, bA0, (s) * 1024); else if constexpr ((s) < 120) ADSR(dst, bA1, ((s) - 60) * 1024); else ADSR(dst, bA2, ((s) - 120) * 1024); } while (0)
    const unsigned wA0 = l16 + lw, wA1 = wA0 + 61440, wA2 = wA0 + 122880;
    constexpr int PD = 10, NS = 37;
    bf16x8 af[NS];
    f32x4 acco[5];
    float gate; u64 fav;
    const unsigned faoff = WS_FA + (rt64 * 8 + (lane & 7)) * 128;
    const char* ap = p.ws + WS_U + (size_t)((rt64 * 4 + wm) * 54 + wk) * 1024;
    const char* op = p.ws + WS_O + (size_t)((rt64 * 4 + wm) * 20 + wk) * 1024;
    sfor<PD>([&](auto I) { constexpr int i = I; gload16<(i & 1) * 2048>(af[i], ap + (i >> 1) * 4096, l16); });
    bf16x8 bfr[5];
    sfor<5>([&](auto T) { constexpr int t = T; BRD(bfr[t], B0 + t); });
    sfor<NS>([&](auto I) {
      constexpr int i = I;
      constexpr auto nA = [](int s) { return (s + PD <= NS ? 1 : 0) + (s == 12 ? 1 : 0); };
      constexpr auto gB = [](int s) { int n = s < 2 ? 0 : (s - 2) + (s - 2) / 4; return n > 28 ? 28 : n; };
      constexpr auto nG = [=](int s) { return gB(s + 1) - gB(s); };
      constexpr auto nB = [=](int s) { int a = gB(s) < NSTG ? NSTG : gB(s), c = gB(s + 1) < NSTG ? NSTG : gB(s + 1); return c - a; };
      constexpr int cntA = [&]() {
        int n = 0;
        if (i < PD) { n = PD - 1 - i; for (int s = 0; s < i; ++s) n += nA(s) + nB(s); }
        else { int s0 = i - PD; n = nB(s0) + (s0 == 12 ? 1 : 0); for (int s = s0 + 1; s < i; ++s) n += nA(s) + nB(s); }
        return n; }();
      constexpr int cntB = [&]() {
        if (10 * (i + 3) <= 150 + 8 * NSTG) return 63;
        int q = (10 * (i + 3) - 150 + 7) / 8 - 1; if (q > 27) q = 27;
        int s0 = 2; while (gB(s0 + 1) <= q) ++s0;
        int n = 0;
        for (int s = s0 + 1; s < i; ++s) n += nA(s) + nB(s);
        return n; }();
      constexpr bool bar = (i & 1) == 0 && i >= 2 && i <= 34;
      if constexpr (i == 17) {
        constexpr int c = nB(12) + nA(13) + nB(13) + nA(14) + nB(14) + nA(15) + nB(15) + nA(16) + nB(16);
        WAITV_R(c, fav);
        if (!__all(fav == tag)) poll_flags(p.ws, faoff, tag);
      }
      WAITV_R((bar && cntB < cntA) ? cntB : cntA, af[i]);
      if constexpr (bar) RAWBAR();
      if constexpr (i == 2 || i == 10 || i == 18 || i == 26) STAMP(22 + i / 8);
      if constexpr (i == 2) { if (tid == 0) set_flag(p.ws, WS_FA + (rt64 * 8 + ct) * 128, tag); }
      sfor<5>([&](auto T) {
        constexpr int t = T;
        if constexpr (i < NS - 1) WAITL(4); else WAITL(4 - t);
        if constexpr (i == 0) AMFMA0(acc[t], bfr[t], af[i]); else if constexpr (i < 27) AMFMA(acc[t], bfr[t], af[i]);
        else if constexpr (i == 27) AMFMA0(acco[t], bfr[t], af[i]); else AMFMA(acco[t], bfr[t], af[i]);
        if constexpr (i < NS - 1) BRD(bfr[t], (B0 + 10 * (i + 1)) % RING + t);
        if constexpr (t == 0) {
          constexpr int n = i + PD;
          if constexpr (n < 27) gload16<(n & 1) * 2048>(af[n], ap + (n >> 1) * 4096, l16);
          else if constexpr (n < NS) gload16<((n - 27) & 1) * 2048>(af[n], op + ((n - 27) >> 1) * 4096, l16);
          else if constexpr (n == NS) gload4<0>(gate, p.ws + WS_GT + (rt64 * 64 + wm * 16) * 16, (unsigned)(r * 16));
        }
        if constexpr (t == 1 && i == 12) gload8<0>(fav, p.ws, faoff);
        if constexpr ((t == 2 && nG(i) >= 1) || (t == 4 && nG(i) == 2)) {
          constexpr int q = gB(i) + (t == 4 ? 1 : 0); constexpr int f = q < 27 ? 150 + 8 * q : 362;
          if constexpr (q < NSTG) {
            constexpr int sl = (B0 + f) % RING;
            if constexpr (sl + 7 < RING) dsw16<(sl % 60) * 1024>(sl < 60 ? wA0 : sl < 120 ? wA1 : wA2, stg[q]);
            else dsw16<0>(l16 + (sl + w >= RING ? sl + w - RING : sl + w) * 1024, stg[q]);
          } else bload<f, B0 + f>(sb4, l16, lw);
        }
      });
    });
    WAITV_R(0, gate);
    MFMA_FENCE5(acc);
    MFMA_FENCE5(acco);
    STAMP(14);
    const float ng = 1.f - gate;
#pragma unroll
    for (int t = 0; t < 5; ++t) acc[t] = gate * acco[t] + ng * acc[t];
    reduce_wk(L + 90 * 1024, wm, wk, lane, acc);
    unsigned o_[3][2];
#pragma unroll
    for (int i = 0; i < 3; ++i) {
      o_[i][0] = pk2(__builtin_fmaf(ng, bflo(x1p[i][0]), acc[i][0]), __builtin_fmaf(ng, bfhi(x1p[i][0]), acc[i][1]));
      o_[i][1] = pk2(__builtin_fmaf(ng, bflo(x1p[i][1]), acc[i][2]), __builtin_fmaf(ng, bfhi(x1p[i][1]), acc[i][3]));
    }
    const __amdgpu_buffer_rsrc_t ro = __builtin_amdgcn_make_buffer_rsrc((void*)p.out, 0, S * D * 2, 0x00020000);
    const v2u a0 = sw16(o_[0][0], o_[1][0]), a1 = sw16(o_[0][1], o_[1][1]), s0 = sw16(o_[2][0], o_[2][0]), s1 = sw16(o_[2][1], o_[2][1]);
    __builtin_amdgcn_raw_buffer_store_b128(v4u{a0[0], a1[0], a0[1], a1[1]}, ro, opair, 0, 16);
    __builtin_amdgcn_raw_buffer_store_b128(v4u{s0[0], s1[0], s0[1], s1[1]}, ro, (wk == 0 && !(g & 1)) ? orow + 128 + (g >> 1) * 16 : OOB, 0, 16);
    STAMP(15);
  }
}

extern "C" void kernel_launch(void* const* d_in, const int* in_sizes, int n_in, void* d_out, int out_size, void* d_ws, size_t ws_size, hipStream_t stream) {
  Params p;
  p.x = (const u16*)d_in[0]; p.wqkv = (const u16*)d_in[1]; p.wo = (const u16*)d_in[2];
  p.w1 = (const u16*)d_in[4]; p.w2 = (const u16*)d_in[5]; p.w3 = (const u16*)d_in[6]; p.w4 = (const u16*)d_in[7]; p.gw = (const u16*)d_in[8];
  p.out = (u16*)d_out; p.ws = (char*)d_ws;
  const int lds = 163840;
  (void)hipFuncSetAttribute((const void*)mega, hipFuncAttributeMaxDynamicSharedMemorySize, lds);
  mega<<<256, 512, lds, stream>>>(p);
}
```

```cpp
#include <hip/hip_runtime.h>
#include <stdint.h>
#include <type_traits>

typedef unsigned short u16;
typedef unsigned long long u64;
typedef short bf16x8 __attribute__((ext_vector_type(8)));
typedef short bf16x4 __attribute__((ext_vector_type(4)));
typedef float f32x4 __attribute__((ext_vector_type(4)));
typedef float f32x2 __attribute__((ext_vector_type(2)));
typedef __bf16 bfv2 __attribute__((ext_vector_type(2)));
typedef unsigned v4u __attribute__((ext_vector_type(4)));
typedef unsigned v2u __attribute__((ext_vector_type(2)));
#define LAS __attribute__((address_space(3)))
#define GAS __attribute__((address_space(1)))
#define DEV __device__ __forceinline__
#define RLX_AGENT __ATOMIC_RELAXED, __HIP_MEMORY_SCOPE_AGENT

constexpr int S = 2048, D = 640, F = 1728;
constexpr unsigned KB = 1024;
constexpr unsigned WS_W4 = 0;
constexpr unsigned WS_QKV = WS_W4 + 2960 * KB;
constexpr unsigned WS_HS = WS_QKV + 7680 * KB;
constexpr unsigned WS_O = WS_HS + 2560 * KB;
constexpr unsigned WS_X1 = WS_O + 2560 * KB;
constexpr unsigned WS_U = WS_X1 + 2560 * KB;
constexpr unsigned WS_ST = WS_U + 6912 * KB;
constexpr unsigned WS_RS = WS_ST + 256 * KB;
constexpr unsigned WS_GT = WS_RS + 32 * KB;
constexpr unsigned WS_CS = WS_GT + 32 * KB;
constexpr unsigned WS_EP = WS_CS + 20 * KB;
constexpr unsigned WS_FW = WS_EP + 1 * KB;
constexpr unsigned WS_FQ = WS_FW + 2 * KB;
constexpr unsigned WS_FH = WS_FQ + 2 * KB;
constexpr unsigned WS_F2 = WS_FH + 16 * KB;
constexpr unsigned WS_FA = WS_F2 + 32 * KB;
constexpr unsigned WS_F3 = WS_FA + 32 * KB;
constexpr unsigned WS_END = WS_F3 + 32 * KB;
constexpr unsigned OOB = 0x40000000u;
constexpr unsigned L_JOB = 104 * KB, L_ATT = 70 * KB, L_CS3 = 156 * KB;
constexpr int B0 = 110;
constexpr int RING = 150;
constexpr unsigned CODE_LINES = 268;

DEV f32x4 mfma(bf16x8 a, bf16x8 b, f32x4 c) { return __builtin_amdgcn_mfma_f32_16x16x32_bf16(a, b, c, 0, 0, 0); }
DEV unsigned pk2(float a, float b) { f32x2 v = {a, b}; bfv2 r = __builtin_convertvector(v, bfv2); return __builtin_bit_cast(unsigned, r); }
DEV float bflo(unsigned d) { return __uint_as_float(d << 16); }
DEV float bfhi(unsigned d) { return __uint_as_float(d & 0xffff0000u); }
DEV float dot2(unsigned a, unsigned b, float c) { return __builtin_amdgcn_fdot2_f32_bf16(__builtin_bit_cast(bfv2, a), __builtin_bit_cast(bfv2, b), c, false); }
DEV float gelu_s(float a, float k1, float k2, float sd) {
  float t = a * __builtin_fmaf(a * a, k2, k1);
  float e = __builtin_amdgcn_exp2f(t);
  return a * __builtin_amdgcn_rcpf(__builtin_fmaf(e, sd, sd));
}
constexpr float GC1 = -2.0f * 0.7978845608028654f * 1.4426950408889634f, GC2 = GC1 * 0.044715f;
DEV void glds(const void* sbase, unsigned voff, unsigned lds) {
  asm volatile("s_mov_b32 m0, %2\n\ts_nop 0\n\tglobal_load_lds_dwordx4 %0, %1 sc1" ::"v"(voff), "s"(sbase), "s"(lds) : "memory");
}
template <int IMM> DEV void glds_same_m0(const void* sbase, unsigned voff) {
  asm volatile("global_load_lds_dwordx4 %0, %1 offset:%2 sc1" ::"v"(voff), "s"(sbase), "n"(IMM) : "memory");
}
template <int IMM, typename T> DEV void gload16(T& d, const void* sbase, unsigned voff) {
  asm volatile("global_load_dwordx4 %0, %1, %2 offset:%3 sc1" : "=v"(d) : "v"(voff), "s"(sbase), "n"(IMM) : "memory");
}
template <int IMM, typename T> DEV void gload8(T& d, const void* sbase, unsigned voff) {
  asm volatile("global_load_dwordx2 %0, %1, %2 offset:%3 sc1" : "=v"(d) : "v"(voff), "s"(sbase), "n"(IMM) : "memory");
}
template <int IMM, typename T> DEV void gload4(T& d, const void* sbase, unsigned voff) {
  asm volatile("global_load_dword %0, %1, %2 offset:%3 sc1" : "=v"(d) : "v"(voff), "s"(sbase), "n"(IMM) : "memory");
}
DEV bf16x8 cat(bf16x4 a, bf16x4 b) { return __builtin_shufflevector(a, b, 0, 1, 2, 3, 4, 5, 6, 7); }
#define WAITV(n) asm volatile("s_waitcnt vmcnt(%0)" ::"n"(n) : "memory")
#define WAITV_R(n, r) asm volatile("s_waitcnt vmcnt(%1)" : "+v"(r) : "n"(n) : "memory")
#define PIN(r) asm volatile("" : "+v"(r))
#define BAR() asm volatile("s_waitcnt lgkmcnt(0)\n\ts_barrier" ::: "memory")
#define RAWBAR() asm volatile("s_barrier" ::: "memory")
#define SB() __builtin_amdgcn_sched_barrier(0)
#define AMFMA(acc, a, b) asm volatile("v_mfma_f32_16x16x32_bf16 %0, %1, %2, %0" : "+v"(acc) : "v"(a), "v"(b))
#define AMFMA0(acc, a, b) asm volatile("v_mfma_f32_16x16x32_bf16 %0, %1, %2, 0" : "=&v"(acc) : "v"(a), "v"(b))
#define ADSR(dst, addr, off) asm volatile("ds_read_b128 %0, %1 offset:%2" : "=v"(dst) : "v"(addr), "n"(off))
#define ADSR8(dst, addr, off) asm volatile("ds_read_b64 %0, %1 offset:%2" : "=v"(dst) : "v"(addr), "n"(off))
#define ADSTR(dst, addr, off) asm volatile("ds_read_b64_tr_b16 %0, %1 offset:%2" : "=v"(dst) : "v"(addr), "n"(off))
#define WAITL(n) asm volatile("s_waitcnt lgkmcnt(%0)" ::"n"(n))
#define TRW(var, R0, R1, R2, R3, addr, off) asm volatile("ds_read_b64_tr_b16 v[" #R0 ":" #R1 "], %1 offset:%2\n\tds_read_b64_tr_b16 v[" #R2 ":" #R3 "], %1 offset:%3" : "={v[" #R0 ":" #R3 "]}"(var) : "v"(addr), "n"(off), "n"((off) + 512))
#define MMW(first, acc, var, R0, R3, x) do { if constexpr (first) asm volatile("v_mfma_f32_16x16x32_bf16 %0, %1, %2, 0" : "=&v"(acc) : "{v[" #R0 ":" #R3 "]}"(var), "v"(x)); \
    else asm volatile("v_mfma_f32_16x16x32_bf16 %0, %1, %2, %0" : "+v"(acc) : "{v[" #R0 ":" #R3 "]}"(var), "v"(x)); } while (0)
#define TRP(R0, R1, R2, R3) asm volatile("ds_read_b64_tr_b16 v[" #R0 ":" #R1 "], %1 offset:%2\n\tds_read_b64_tr_b16 v[" #R2 ":" #R3 "], %1 offset:%3" : "={v[" #R0 ":" #R3 "]}"(d) : "v"(a), "n"(O1), "n"(O2))
template <int T, int O1, int O2> DEV void trb(bf16x8& d, unsigned a) {
  if constexpr (T == 0) TRP(100, 101, 102, 103); else if constexpr (T == 1) TRP(104, 105, 106, 107); else if constexpr (T == 2) TRP(108, 109, 110, 111); else if constexpr (T == 3) TRP(112, 113, 114, 115);
  else if constexpr (T == 4) TRP(116, 117, 118, 119); else if constexpr (T == 5) TRP(120, 121, 122, 123); else TRP(124, 125, 126, 127);
}
template <int T, bool FI> DEV void mmb(f32x4& acc, bf16x8& wv, bf16x8& x) {
  if constexpr (T == 0) MMW(FI, acc, wv, 100, 103, x); else if constexpr (T == 1) MMW(FI, acc, wv, 104, 107, x); else if constexpr (T == 2) MMW(FI, acc, wv, 108, 111, x); else if constexpr (T == 3) MMW(FI, acc, wv, 112, 115, x);
  else if constexpr (T == 4) MMW(FI, acc, wv, 116, 119, x); else if constexpr (T == 5) MMW(FI, acc, wv, 120, 123, x); else MMW(FI, acc, wv, 124, 127, x);
}
template <typename T> DEV void pin(T& v) { PIN(v); }
template <int OFF, typename T> DEV void dstr(T& d, unsigned a) { ADSTR(d, a, OFF); }
template <int OFF> DEV void dsw16(unsigned a, v4u v) { asm volatile("ds_write_b128 %0, %1 offset:%2" ::"v"(a), "v"(v), "n"(OFF) : "memory"); }
template <int OFF, typename T> DEV void dsr16(T& d, unsigned a) { ADSR(d, a, OFF); }
DEV void bstore16(v4u v, v4u rq, unsigned voff, unsigned soff) {
  asm volatile("buffer_store_dwordx4 %0, %1, %2, %3 offen sc1\n\ts_nop 1" ::"v"(v), "v"(voff), "s"(rq), "s"(soff) : "memory");
}
#define BSTORE16(v, voff, soff) bstore16(v, rq, voff, soff)

#define STAMP(i) do {} while (0)

struct Params {
  const u16 *x, *wqkv, *wo, *w1, *w2, *w3, *w4, *gw;
  u16* out;
  char* ws;
};

template <int V> using IC = std::integral_constant<int, V>;
template <int N, typename Fn> DEV void sfor_impl(Fn&& f) { if constexpr (N > 0) { sfor_impl<N - 1>(f); f(IC<N - 1>{}); } }
template <int N, typename Fn> DEV void sfor(Fn&& f) { sfor_impl<N>(f); }

DEV float xsum4(float v) {
  unsigned u = __float_as_uint(v);
  auto a = __builtin_amdgcn_permlane16_swap(u, u, false, false);
  v = __uint_as_float(a[0]) + __uint_as_float(a[1]);
  u = __float_as_uint(v);
  auto b = __builtin_amdgcn_permlane32_swap(u, u, false, false);
  return __uint_as_float(b[0]) + __uint_as_float(b[1]);
}
DEV float xmax4(float v) {
  unsigned u = __float_as_uint(v);
  auto a = __builtin_amdgcn_permlane16_swap(u, u, false, false);
  v = fmaxf(__uint_as_float(a[0]), __uint_as_float(a[1]));
  u = __float_as_uint(v);
  auto b = __builtin_amdgcn_permlane32_swap(u, u, false, false);
  return fmaxf(__uint_as_float(b[0]), __uint_as_float(b[1]));
}
DEV v2u sw16(unsigned a, unsigned b) { auto t = __builtin_amdgcn_permlane16_swap(a, b, false, false); return v2u{t[0], t[1]}; }
DEV unsigned hi32(unsigned u) { auto b = __builtin_amdgcn_permlane32_swap(u, u, false, false); return b[1]; }
template <int CTRL> DEV float dpp_add(float v) { return v + __uint_as_float(__builtin_amdgcn_update_dpp(0, __float_as_uint(v), CTRL, 0xf, 0xf, true)); }
DEV float sum32(float v) {
  v = dpp_add<0xB1>(v); v = dpp_add<0x4E>(v); v = dpp_add<0x141>(v); v = dpp_add<0x140>(v);
  unsigned u = __float_as_uint(v);
  auto a = __builtin_amdgcn_permlane16_swap(u, u, false, false);
  return __uint_as_float(a[0]) + __uint_as_float(a[1]);
}
DEV float sum8(float v) { v = dpp_add<0xB1>(v); v = dpp_add<0x4E>(v); return dpp_add<0x141>(v); }

DEV void poll_flags(const char* ws, unsigned off, u64 tag) {
  GAS u64* f = (GAS u64*)(ws + off);
#pragma unroll 1
  for (unsigned spins = 0; spins < (1u << 16); ++spins) {
    u64 v = __hip_atomic_load(f, RLX_AGENT);
    if (__all(v == tag)) break;
    __builtin_amdgcn_s_sleep(1);
  }
}
DEV void set_flag(char* ws, unsigned off, u64 tag) { __hip_atomic_store((GAS u64*)(ws + off), tag, RLX_AGENT); }

DEV void reduce_wk(LAS char* redb, int wm, int wk, int lane, f32x4 (&acc)[5]) {
  LAS f32x4* red = (LAS f32x4*)redb + wm * 5 * 64 + lane;
  if (wk == 0) { red[2 * 64] = acc[2]; red[3 * 64] = acc[3]; }
  else { red[0] = acc[0]; red[64] = acc[1]; red[4 * 64] = acc[4]; }
  BAR();
  if (wk == 0) { acc[0] += red[0]; acc[1] += red[64]; acc[2] = acc[4] + red[4 * 64]; }
  else { acc[0] = acc[2] + red[2 * 64]; acc[1] = acc[3] + red[3 * 64]; acc[2] = f32x4{0, 0, 0, 0}; }
}
#define MFMA_FENCE5(a) asm volatile("s_nop 7\n\ts_nop 1" : "+v"(a[0]), "+v"(a[1]), "+v"(a[2]), "+v"(a[3]), "+v"(a[4]))

template <int FRAG, int POS> DEV void bload(const char* sbw, unsigned l16, unsigned lw) {
  constexpr int sl = POS % RING;
  if constexpr (sl + 7 < RING) glds(sbw + (size_t)FRAG * 1024, l16, lw + sl * 1024u);
  else { unsigned d = lw + sl * 1024u; if (d >= RING * 1024u) d -= RING * 1024u; glds(sbw + (size_t)FRAG * 1024, l16, d); }
}

__global__ __launch_bounds__(512, 2) void mega(Params p) {
  extern __shared__ __attribute__((aligned(16))) char smem[];
  LAS char* L = (LAS char*)smem;
  u64 pc0;
  asm volatile("s_getpc_b64 %0" : "=s"(pc0));
  pc0 &= ~255ull;
  const int b = blockIdx.x, tid = threadIdx.x, lane = tid & 63;
  const int w = __builtin_amdgcn_readfirstlane(tid >> 6);
  const int xcd = b & 7, j = b >> 3, grp = xcd >> 1, half = xcd & 1;
  const int rt128 = 4 * grp + (j & 3), sidx = j >> 2, cg = 8 * half + sidx;
  const int rt64 = 8 * grp + (j & 7), ct = 4 * half + (j >> 3);
  const int wm = w & 3, wk = w >> 2, r = lane & 15, g = lane >> 4;
  const unsigned l16 = lane * 16, lw = w * 1024;
  const __amdgpu_buffer_rsrc_t rs = __builtin_amdgcn_make_buffer_rsrc((void*)p.ws, 0, WS_END, 0x00020000);
  const v4u rq = {(unsigned)(size_t)p.ws, (unsigned)((size_t)p.ws >> 32) & 0xffffu, WS_END, 0x00020000u};
  const char* sb2 = (const char*)p.w2 + ct * 160;
  unsigned v2[5];
  auto w2init = [&]() {
#pragma unroll
    for (int k = 0; k < 5; ++k) { const unsigned u = (8 * k + w) * 64 + lane, row = (u * 6554) >> 16; v2[k] = row * 1120 + u * 16; PIN(v2[k]); }
  };
  auto w2load = [&](auto N) {
    constexpr int n = N;
    if constexpr (n < 12) glds(sb2 + (size_t)(n / 5) * 256 * 1280, v2[n % 5], lw + n * 8192);
    else glds(sb2 + (size_t)(w < 4 ? 512 : 256) * 1280, v2[2], lw + n * 8192);
  };
  v4u qp[6];
  u64 tag; unsigned tag32;
  v2u ep;
  STAMP(0);
  auto make_tag = [&]() {
    unsigned e0, e1;
    asm volatile("v_readfirstlane_b32 %0, %2\n\tv_readfirstlane_b32 %1, %3\n\ts_nop 3" : "=s"(e0), "=s"(e1) : "v"(ep[0]), "v"(ep[1]));
    const u64 e = ((u64)e1 << 32) | e0;
    tag = e * 6364136223846793005ull + 1442695040888963407ull; tag32 = (unsigned)(tag >> 32);
  };
  auto epload = [&]() {
    unsigned ln = (j * 8 + w) * 2 + (lane & 1); if (ln >= CODE_LINES) ln = CODE_LINES - 1;
    const u64 a = (lane == 1 || lane == 2) ? pc0 + ln * 128 : (u64)(p.ws + WS_EP);
    asm volatile("global_load_dwordx2 %0, %1, off sc1" : "=v"(ep) : "v"(a) : "memory");
  };
  const int role = sidx < 5 ? 0 : cg != 15 ? 1 : 2;
  unsigned fhoff;
  if (__builtin_expect(role != 2, 1)) {
    const bool isq = role == 0;
    const int wm2 = w & 1, wn = w >> 1;
    const int col = isq ? 5 * half + sidx : 3 * half + sidx - 5;
    const int pitch = isq ? 3840 : 1280;
    const char* sX[5];
    sX[0] = (const char*)p.x + (size_t)rt128 * 128 * 1280; sX[1] = sX[0] + 64 * 1280;
    sX[2] = (const char*)(isq ? p.wqkv : p.w1) + col * (isq ? 384 : 256); sX[3] = sX[2] + 128; sX[4] = sX[2] + 256;
    int kc = sidx + ((5 * (rt128 & 3) + 1) >> 1); if (kc >= 10) kc -= 10;
    unsigned va, vb;
    {
      const int srow = lane >> 3, ss = lane & 7, r0 = 8 * w + srow;
      va = r0 * 1280 + ((ss ^ ((r0 >> 1) & 7)) << 4) + kc * 128;
      const int swz = ((r0 >> 1) & 1) | (((r0 >> 3) & 1) << 1);
      vb = (r0 + kc * 64) * pitch + (((((ss >> 1) ^ swz) << 1) | (ss & 1)) << 4);
    }
    auto advance = [&]() {
      va += kc == 9 ? -9 * 128 : 128; vb += (kc == 9 ? -9 * 64 : 64) * pitch;
      kc = kc == 9 ? 0 : kc + 1;
    };
    epload();
    v4u sx[3], sg[3];
    if (isq) {
      const unsigned vg = (lane & 31) * 16, vs = ((col & 7) * 16 + 2 * w + (lane >> 5)) * 1280 + vg;
      gload16<0>(sx[0], sX[0], vs); gload16<512>(sx[1], sX[0], vs); gload16<1024>(sx[2], sX[0], vs - (lane & 16) * 16);
      gload16<0>(sg[0], p.gw, vg); gload16<512>(sg[1], p.gw, vg); gload16<1024>(sg[2], p.gw, (unsigned)(r * 16));
    }
    sfor<2>([&](auto C) {
      constexpr int c = C; constexpr unsigned sl = ((c + 2) % 3) * 40960;
      sfor<4>([&](auto K) { constexpr int k = K; glds(sX[k], k < 2 ? va : vb, lw + sl + k * 8192); });
      if (isq) glds(sX[4], vb, lw + sl + 4 * 8192);
      advance();
    });
    {
      const int own = rt64 >> 1, nb = (own + ((rt64 & 1) ? 1 : 15)) & 15;
      const int li = lane < 10 ? lane : 0;
      fhoff = WS_FH + (li < 5 ? own * 8 + li : nb * 8 + li - 5) * 128;
    }
    auto s1run = [&](auto NT_) {
      constexpr int NT = NT_, NG = 2 + NT; constexpr bool ISQ = NT == 3;
      unsigned mslot = 40960 + lw;
      auto issue = [&](auto K) {
        constexpr int k = K;
        if constexpr (k >= 100) w2load(IC<k - 100>{});
        else {
          glds(sX[k], k < 2 ? va : vb, mslot + k * 8192);
          if constexpr (k == NG - 1) { advance(); mslot += 40960; if (mslot >= 122880) mslot -= 122880; }
        }
      };
      sfor<NG>(issue);
      w2init();
      unsigned xo[2], wo_[NT];
      xo[0] = 81920 + wm2 * 8192 + r * 128 + ((g ^ (r >> 1)) << 4);
      xo[1] = xo[0] ^ 64;
      {
        const int q = (lane >> 2) & 3, pp = lane & 3, swz = ((q >> 1) & 1) | ((g & 1) << 1);
#pragma unroll
        for (int t = 0; t < NT; ++t) { const int U = NT * wn + t; wo_[t] = 81920 + 16384 + (U >> 2) * 8192 + (8 * g + q) * 128 + (((U & 3) ^ swz) << 5) + pp * 8; PIN(wo_[t]); }
      }
      PIN(xo[0]); PIN(xo[1]); PIN(va); PIN(vb); PIN(fhoff);
      STAMP(30);
      SB();
      if constexpr (ISQ) {
        WAITV(15);
        make_tag();
        PIN(sx[0]); PIN(sx[1]); PIN(sx[2]); PIN(sg[0]); PIN(sg[1]); PIN(sg[2]);
        float sm = 0, sq = 0, gt = 0, sm2 = 0, sq2 = 0, gt2 = 0;
#pragma unroll
        for (int e = 0; e < 4; ++e) {
          sm = dot2(sx[0][e], 0x3F803F80u, sm); sq = dot2(sx[0][e], sx[0][e], sq); gt = dot2(sx[0][e], sg[0][e], gt);
          sm2 = dot2(sx[2][e], 0x3F803F80u, sm2); sq2 = dot2(sx[2][e], sx[2][e], sq2); gt2 = dot2(sx[2][e], sg[2][e], gt2);
          sm = dot2(sx[1][e], 0x3F803F80u, sm); sq = dot2(sx[1][e], sx[1][e], sq); gt = dot2(sx[1][e], sg[1][e], gt);
        }
        sm = sum32(__builtin_fmaf(sm2, 0.5f, sm)); sq = sum32(__builtin_fmaf(sq2, 0.5f, sq)); gt = sum32(__builtin_fmaf(gt2, 0.5f, gt));
        const float mean = sm * (1.0f / D);
        const float rstd = __builtin_amdgcn_rsqf(fmaxf(sq * (1.0f / D) - mean * mean, 0.0f) + 1e-5f);
        const float gate = __builtin_amdgcn_rcpf(1.0f + __expf(-gt));
        BSTORE16((v4u{__float_as_uint((lane & 1) ? gate : mean), tag32, __float_as_uint(rstd), tag32}), ((lane & 30) == 0 && col < 8) ? (lane >> 5) * 16 + (lane & 1) * (WS_GT - WS_RS) : OOB,
                 WS_RS + (rt128 * 128 + (col & 7) * 16 + 2 * w) * 16);
        SB();
        WAITV(11);
      } else WAITV(2 * NG);
      RAWBAR();
      STAMP(1);
      f32x4 acc[NT][4];
      bf16x8 xf0[4], xf1[4], wa0, wa1, wa2, wb0, wb1, wb2;
      int dnext = -2 * 40960;
#define MM_(t, m) do { if constexpr (P == 0) { if constexpr (t == 0) MMW(fi, acc[0][m], wa0, 100, 103, xf0[m]); else if constexpr (t == 1) MMW(fi, acc[1][m], wa1, 104, 107, xf0[m]); else MMW(fi, acc[NT - 1][m], wa2, 108, 111, xf0[m]); } \
    else { if constexpr (t == 0) MMW(fi, acc[0][m], wb0, 112, 115, xf1[m]); else if constexpr (t == 1) MMW(fi, acc[1][m], wb1, 116, 119, xf1[m]); else MMW(fi, acc[NT - 1][m], wb2, 120, 123, xf1[m]); } } while (0)
#define RW_(P, t) do { if constexpr (P == 1) { if constexpr (t == 0) TRW(wa0, 100, 101, 102, 103, wo_[0], 0); else if constexpr (t == 1) TRW(wa1, 104, 105, 106, 107, wo_[1], 0); else TRW(wa2, 108, 109, 110, 111, wo_[NT - 1], 0); } \
    else { if constexpr (t == 0) TRW(wb0, 112, 113, 114, 115, wo_[0], 4096); else if constexpr (t == 1) TRW(wb1, 116, 117, 118, 119, wo_[1], 4096); else TRW(wb2, 120, 121, 122, 123, wo_[NT - 1], 4096); } } while (0)
#define RX_(P, m) do { if constexpr (P == 1) ADSR(xf0[m], xo[0], (m) * 2048); else ADSR(xf1[m], xo[1], (m) * 2048); } while (0)
      auto kstep = [&](auto P_, auto G0, auto GN, auto RD, auto UPD, auto FIRST) {
        constexpr int P = P_, g0 = G0, gn = GN; constexpr bool rd = RD, upd = UPD, fi = FIRST;
        MM_(0, 0); if constexpr (rd) RW_(P, 0);
        MM_(0, 1); if constexpr (rd) RX_(P, 0);
        MM_(0, 2); if constexpr (rd) RX_(P, 1);
        MM_(0, 3); if constexpr (rd) { RX_(P, 2); if constexpr (NT == 2) RX_(P, 3); }
        MM_(1, 0); if constexpr (rd) { if constexpr (NT == 3) RX_(P, 3); else RW_(P, 1); }
        if constexpr (NT == 2 && gn > 0) issue(IC<g0>{});
        MM_(1, 1); if constexpr (rd && NT == 3) RW_(P, 1);
        MM_(1, 2); if constexpr (rd && NT == 3) RW_(P, 2);
        if constexpr (NT == 2 && gn > 1) issue(IC<g0 + 1>{});
        if constexpr (NT == 3 && gn > 0) issue(IC<g0>{});
        MM_(1, 3);
        if constexpr (NT == 3) {
          MM_(2, 0);
          if constexpr (gn > 1) issue(IC<g0 + 1>{});
          MM_(2, 1); MM_(2, 2);
          if constexpr (gn > 2) issue(IC<g0 + 2>{});
          if constexpr (upd) { wo_[0] += dnext; wo_[1] += dnext; wo_[NT - 1] += dnext; xo[0] += dnext; xo[1] += dnext; }
          SB();
          MM_(2, 3);
        } else {
          if constexpr (upd) { wo_[0] += dnext; wo_[1] += dnext; xo[0] += dnext; xo[1] += dnext; }
        }
        SB();
      };
      using BT = std::true_type; using BF = std::false_type;
      RW_(1, 0); RX_(1, 0); RX_(1, 1); RX_(1, 2); RX_(1, 3); RW_(1, 1); if constexpr (NT == 3) RW_(1, 2);
      WAITL(0);
      SB();
      kstep(IC<0>{}, IC<0>{}, IC<0>{}, BT{}, BT{}, BT{});
      if constexpr (ISQ) {
        auto iter = [&](auto G0, auto LAST) {
          constexpr int g0 = G0;
          BAR();
          kstep(IC<1>{}, IC<g0>{}, IC<3>{}, BT{}, BF{}, BF{});
          WAITL(0);
          kstep(IC<0>{}, IC<g0 + 3>{}, IC<2>{}, BT{}, std::integral_constant<bool, !LAST>{}, BF{});
        };
        dnext = 40960;
        WAITV(6); iter(IC<0>{}, BF{});
        WAITV(6); iter(IC<0>{}, BF{});
#pragma unroll 1
        for (int ch = 2; ch < 7; ++ch) { dnext = (ch == 2 || ch == 5) ? -2 * 40960 : 40960; WAITV(5); iter(IC<0>{}, BF{}); }
        WAITV(5); iter(IC<100>{}, BF{});
        WAITV(5); iter(IC<105>{}, BT{});
        BAR();
        kstep(IC<1>{}, IC<110>{}, IC<3>{}, BF{}, BF{}, BF{});
        asm volatile("s_nop 7\n\ts_nop 1" : "+v"(acc[2][0]), "+v"(acc[2][1]), "+v"(acc[2][2]), "+v"(acc[2][3]));
        STAMP(2);
#pragma unroll
        for (int t = 0; t < 3; ++t)
#pragma unroll
          for (int mp = 0; mp < 2; ++mp) {
            PIN(acc[t][2 * mp]); PIN(acc[t][2 * mp + 1]);
            f32x4 a = acc[t][2 * mp], bb = acc[t][2 * mp + 1];
            qp[2 * t + mp] = v4u{pk2(a[0], a[1]), pk2(a[2], a[3]), pk2(bb[0], bb[1]), pk2(bb[2], bb[3])};
          }
        STAMP(3);
      } else {
        unsigned rslot = 0;
#pragma unroll 1
        for (int ch = 0; ch < 7; ++ch) {
          dnext = rslot == 2 ? -2 * 40960 : 40960;
          rslot = rslot == 2 ? 0 : rslot + 1;
          WAITV(NG);
          BAR();
          kstep(IC<1>{}, IC<0>{}, IC<NT>{}, BT{}, BF{}, BF{});
          WAITL(0);
          kstep(IC<0>{}, IC<NT>{}, IC<2>{}, BT{}, BT{}, BF{});
        }
        make_tag();
        dnext = 40960;
        WAITV(NG);
        BAR();
        v4u st[4], cs[4];
        const unsigned sto = WS_RS + (rt128 * 128 + wm2 * 64) * 16, cso = WS_CS + (col * 128 + wn * 32) * 8;
        sfor<4>([&](auto M) { constexpr int m = M; gload16<m * 256>(st[m], p.ws + sto, (unsigned)(r * 16)); });
        kstep(IC<1>{}, IC<0>{}, IC<0>{}, BT{}, BF{}, BF{});
        sfor<4>([&](auto M) { constexpr int m = M; gload16<(m >> 1) * 128 + (m & 1) * 16>(cs[m], p.ws + cso, (unsigned)(g * 32)); });
        WAITL(0);
        kstep(IC<0>{}, IC<100>{}, IC<1>{}, BT{}, BT{}, BF{});
        WAITV(9);
        BAR();
        kstep(IC<1>{}, IC<101>{}, IC<2>{}, BT{}, BF{}, BF{});
        WAITL(0);
        kstep(IC<0>{}, IC<103>{}, IC<1>{}, BT{}, BF{}, BF{});
        BAR();
        kstep(IC<1>{}, IC<104>{}, IC<2>{}, BF{}, BF{}, BF{});
        STAMP(2);
        asm volatile("s_waitcnt vmcnt(6)\n\ts_nop 7" : "+v"(st[0]), "+v"(st[1]), "+v"(st[2]), "+v"(st[3]), "+v"(cs[0]), "+v"(cs[1]), "+v"(cs[2]), "+v"(cs[3])::"memory");
#pragma unroll 1
        for (unsigned spins = 0; spins < (1u << 16); ++spins) {
          unsigned bad = 0;
#pragma unroll
          for (int m = 0; m < 4; ++m) bad |= (st[m][1] ^ tag32) | (st[m][3] ^ tag32) | (cs[m][1] ^ tag32) | (cs[m][3] ^ tag32);
          if (__all(bad == 0)) break;
#pragma unroll
          for (int m = 0; m < 4; ++m) {
            st[m] = __builtin_amdgcn_raw_buffer_load_b128(rs, r * 16 + m * 256, sto, 16);
            cs[m] = __builtin_amdgcn_raw_buffer_load_b128(rs, g * 32 + (m >> 1) * 128 + (m & 1) * 16, cso, 16);
          }
        }
        STAMP(31);
        const int hp = 4 * col + wn;
        const int gq = (hp * 13) >> 6;
        const int delta = gq == 0 ? 1 : gq == 1 ? -1 : gq == 2 ? 2 : -2;
        sfor<4>([&](auto M_) {
          constexpr int m = M_;
          const float mean = __uint_as_float(st[m][0]), rstd = __uint_as_float(st[m][2]);
          const float k1 = GC1 * rstd, k2 = GC2 * rstd * rstd * rstd, sd = __builtin_amdgcn_rcpf(rstd);
          int drow = rt128 * 128 + wm2 * 64 + m * 16 + r + delta;
          const bool wrap = (unsigned)drow >= (unsigned)S;
          drow &= (S - 1);
          const unsigned fb = (drow >> 4) * 20 * 1024 + (g * 16 + (drow & 15)) * 16;
          v4u o;
#pragma unroll
          for (int t = 0; t < 2; ++t) {
            pin(acc[t][m]);
#pragma unroll
            for (int h = 0; h < 2; ++h) {
              float h0 = gelu_s(__builtin_fmaf(-mean, __uint_as_float(cs[2 * t + h][0]), acc[t][m][2 * h]), k1, k2, sd);
              float h1 = gelu_s(__builtin_fmaf(-mean, __uint_as_float(cs[2 * t + h][2]), acc[t][m][2 * h + 1]), k1, k2, sd);
              o[2 * t + h] = wrap ? 0u : pk2(h0, h1);
            }
          }
          BSTORE16(o, fb, WS_HS + hp * 1024);
          SB();
        });
        STAMP(29);
        WAITV(0);
        RAWBAR();
        if (tid == 0) set_flag(p.ws, WS_FH + (rt128 * 8 + col) * 128, tag);
        STAMP(3);
        sfor<7>([&](auto I) { w2load(IC<6 + I>{}); });
      }
    };
    if (!isq) s1run(IC<2>{}); else s1run(IC<3>{});
  } else {
    const int z = rt128;
    {
      const int own = rt64 >> 1, nb = (own + ((rt64 & 1) ? 1 : 15)) & 15;
      const int li = lane < 10 ? lane : 0;
      fhoff = WS_FH + (li < 5 ? own * 8 + li : nb * 8 + li - 5) * 128;
    }
    epload();
    v4u selu;
#pragma unroll
    for (int e = 0; e < 4; ++e) selu[e] = r == 2 * e ? 0x3F80u : r == 2 * e + 1 ? 0x3F800000u : 0u;
    const bf16x8 sel = __builtin_bit_cast(bf16x8, selu);
    w2init();
    v4u cr[3][10];
    auto zload = [&](auto K) {
      constexpr int k = K;
      const int task = z + 16 * k < 37 ? z + 16 * k : z;
      const int tp = task < 10 ? 1280 : 3456;
      const char* src = (task < 10 ? (const char*)p.w1 + task * 128 : (const char*)p.w3 + (task - 10) * 128) + (size_t)w * 80 * tp;
      const unsigned vx = (lane >> 3) * tp + (lane & 7) * 16;
      if (k < 2 || z < 5) sfor<10>([&](auto I) { constexpr int i = I; gload16<0>(cr[k][i], src + (size_t)i * 8 * tp, vx); });
      else sfor<10>([&](auto I) {
        constexpr int i = I; gload16<0>(cr[k][i], (const char*)p.w2 + (size_t)(z - 5) * 73728 + i * 8192, l16 + lw); });
    };
    zload(IC<0>{}); zload(IC<1>{});
    sfor<3>([&](auto K) {
      constexpr int k = K;
      LAS float* cp = (LAS float*)(L + k * 4096);
      if constexpr (k == 1) zload(IC<2>{});
      WAITV(k < 2 ? 10 : 0);
      if constexpr (k == 0) make_tag();
      f32x4 c = {0, 0, 0, 0};
#pragma unroll
      for (int i = 0; i < 10; ++i) { pin(cr[k][i]); c = mfma(sel, __builtin_bit_cast(bf16x8, cr[k][i]), c); }
      if (g < 2) *(LAS f32x4*)(cp + (w * 2 + (r >> 3)) * 64 + (r & 7) * 8 + g * 4) = c;
      BAR();
      if (w == 1 + k) {
        f32x2 s = {0, 0};
#pragma unroll
        for (int i = 0; i < 16; ++i) s += *(LAS f32x2*)(cp + i * 64 + (lane & 31) * 2);
        BSTORE16((v4u{__float_as_uint(s[0]), tag32, __float_as_uint(s[1]), tag32}), (lane < 32 && z + 16 * k < 37) ? l16 : OOB, WS_CS + (z + 16 * k) * 512);
      }
      STAMP(1 + k);
    });
    BAR();
    sfor<13>(w2load);
  }
  v4u xr; v2u xs;
  const unsigned orow = (unsigned)(rt64 * 64 + wm * 16 + r) * 1280 + ct * 160;
  const unsigned opair = orow + wk * 64 + (g & 1) * 32 + (g >> 1) * 16;
  const int q4 = (lane >> 2) & 3, pp = lane & 3;
  unsigned v3[7];
  const int cmax3 = cg < 12 ? 13 : 11;
#pragma unroll
  for (int k = 0; k < 7; ++k) {
    const unsigned u = (8 * k + w) * 64 + lane, row = (u * 4682) >> 16; unsigned ch = u - row * 14; if (ch > cmax3) ch = cmax3;
    v3[k] = (row + (row >> 6) * 16) * 3456 + ch * 16; PIN(v3[k]);
  }
  const char* sb3 = (const char*)p.w3 + (6 * cg + (cg < 12 ? cg : 12)) * 32;
  unsigned v3c[4];
#pragma unroll
  for (int k = 0; k < 4; ++k) {
    const unsigned u = (8 * k + (k == 3 ? (w & 3) : w)) * 64 + lane, row = (u * 4682) >> 16; unsigned ch = u - row * 14; if (ch > cmax3) ch = cmax3;
    v3c[k] = (160 * (row >> 5) + 64 + 80 * ((row >> 3) & 1) + 8 * ((row >> 2) & 1) + 4 * ((row >> 4) & 1) + (row & 3)) * 3456 + ch * 16; PIN(v3c[k]);
  }
  v4u w3s[7];
  auto w3load = [&](auto N) {
    constexpr int n = N;
    const unsigned dst = n < 4 ? lw + (120 + 8 * n) * 1024 : n > 4 ? lw + (8 * n - 35) * 1024 : (w < 3 ? lw + 152 * 1024 : lw - 3 * 1024);
    if constexpr (n < 11) glds(sb3 + (size_t)(n / 7) * 320 * 3456, v3[n % 7], dst);
    else if constexpr (n < 14) gload16<0>(w3s[n - 11], sb3 + (size_t)320 * 3456, v3[n % 7]);
    else gload16<0>(w3s[n - 11], sb3, v3c[n - 14]);
  };
  const char* sb4 = p.ws + WS_W4 + ct * 370 * 1024 + lw;
  sfor<4>(w3load);
  if (w == 7) poll_flags(p.ws, fhoff, tag);
  WAITV(4);
  gload16<0>(xr, p.x, opair);
  gload8<128>(xs, p.x, orow + g * 8);
  RAWBAR();
  STAMP(4);
  f32x4 acc[5];
  unsigned x1p[3][2];
  v2u jf[4];
  {
    bf16x8 af[10];
    const char* ap = p.ws + WS_HS + (size_t)((rt64 * 4 + wm) * 20 + wk) * 1024;
    sfor<10>([&](auto I) { constexpr int i = I; gload16<(i & 1) * 2048>(af[i], ap + (i >> 1) * 4096, l16); });
    {
      const int e = (lane >= 10) + (lane >= 20) + (lane >= 30);
      const unsigned vo = e * 1120 + l16;
      const int k0 = xcd * 4 + (j >> 3);
      if (lane < 40) {
#pragma unroll
        for (int i = 0; i < 2; ++i) {
          int ks = k0 + 32 * i; if (ks >= 54) ks = 0;
          const int c3 = (ks * 43) >> 7;
          int R = 16 * (6 * c3 + (c3 < 12 ? c3 : 12)) + 32 * (ks - 3 * c3) + (w & 1) * 16 + (w >> 1) * 4;
          if (ks >= 48) R = 224 * (ks - 48) + 96 + (w & 3) * 4 + (w >> 2) * 112;
          glds((const char*)p.w4 + (size_t)R * 1280 + (j & 7) * 160, vo, L_JOB + i * 5120 + w * 640);
        }
        const int ks = k0 < 20 ? k0 : 0;
        const int R = ks < 16 ? 40 * ks + (w & 1) * 20 + (w >> 1) * 4 : 160 * (ks - 16) + 16 + 20 * w;
        glds((const char*)p.wo + (size_t)R * 1280 + (j & 7) * 160, vo, L_JOB + 10240 + w * 640);
      }
    }
    bf16x8 bfr[5];
    const unsigned b0 = wk * 5120 + (4 * g + q4) * 160 + pp * 8, b1 = b0 + 51200;
    sfor<5>([&](auto T) { constexpr int t = T; trb<t, t * 32, t * 32 + 2560>(bfr[t], b0); });
    sfor<10>([&](auto I) {
      constexpr int i = I;
      WAITV_R(9 - i + 3 + (i > 2 ? i - 2 : 0), af[i]);
      if constexpr (i == 5) RAWBAR();
      sfor<5>([&](auto T) {
        constexpr int t = T;
        if constexpr (i < 9) WAITL(8); else WAITL(8 - 2 * t);
        mmb<t, i == 0>(acc[t], bfr[t], af[i]);
        if constexpr (i < 9) { constexpr int o = ((i + 1) % 5) * 10240 + t * 32; trb<t, o, o + 2560>(bfr[t], i + 1 < 5 ? b0 : b1); }
        if constexpr (t == 2 && i >= 2 && i < 9) w3load(IC<(i < 9 ? 9 + i : 11)>{});
      });
    });
    WAITV(7);
    MFMA_FENCE5(acc);
    STAMP(5);
    reduce_wk(L, wm, wk, lane, acc);
    {
      const int f1 = w < 7 ? 8 + w : 14;
      const unsigned ja = L_JOB + g * 1280 + q4 * 160 + pp * 8;
      const unsigned j0 = ja + (w < 5 ? w * 32 : 5120 + (w - 5) * 32), j1 = ja + (f1 < 10 ? 5120 + (f1 - 5) * 32 : 10240 + (f1 - 10) * 32);
      SB();
      ADSTR(jf[0], j0, 0); ADSTR(jf[1], j0, 640); ADSTR(jf[2], j1, 0); ADSTR(jf[3], j1, 640);
    }
    PIN(xr); PIN(xs);
    const v2u xa = sw16(xr[0], xr[2]), xb = sw16(xr[1], xr[3]);
    float sum = 0, sq = 0;
#pragma unroll
    for (int i = 0; i < 3; ++i) {
      const unsigned x0 = i < 2 ? xa[i] : xs[0], x1 = i < 2 ? xb[i] : xs[1];
      x1p[i][0] = pk2(bflo(x0) + acc[i][0], bfhi(x0) + acc[i][1]); x1p[i][1] = pk2(bflo(x1) + acc[i][2], bfhi(x1) + acc[i][3]);
      if (i < 2 || wk == 0) {
        sum = dot2(x1p[i][0], 0x3F803F80u, sum); sum = dot2(x1p[i][1], 0x3F803F80u, sum);
        sq = dot2(x1p[i][0], x1p[i][0], sq); sq = dot2(x1p[i][1], x1p[i][1], sq);
      }
    }
    const unsigned so = WS_X1 + (rt64 * 4 + wm) * 20 * 1024;
    BSTORE16((v4u{x1p[0][0], x1p[0][1], x1p[1][0], x1p[1][1]}), l16, so + (2 * ct + wk) * 1024);
    const v2u s0 = sw16(x1p[2][0], x1p[2][0]), s1 = sw16(x1p[2][1], x1p[2][1]);
    BSTORE16((v4u{s0[0], s1[0], s0[1], s1[1]}), (wk == 0 && !(g & 1)) ? (g >> 1) * 256 + r * 16 : OOB, so + 16 * 1024 + ct * 512);
    sum = xsum4(sum); sq = xsum4(sq);
    __builtin_amdgcn_raw_buffer_store_b64(__builtin_bit_cast(v2u, f32x2{sum, sq}), rs, g == 0 ? r * 8 : OOB, WS_ST + ((rt64 * 4 + wm) * 16 + ct * 2 + wk) * 128, 16);
  }
  {
    asm volatile("s_waitcnt lgkmcnt(0)" : "+v"(jf[0]), "+v"(jf[1]), "+v"(jf[2]), "+v"(jf[3])::"memory");
    const int k0 = xcd * 4 + (j >> 3), f1 = w < 7 ? 8 + w : 14;
    const unsigned d0 = WS_W4 + ((j & 7) * 74 + k0) * 5120;
    BSTORE16((v4u{jf[0][0], jf[0][1], jf[1][0], jf[1][1]}), (w < 5 || k0 < 22) ? l16 : OOB, d0 + (w < 5 ? w * 1024 : 32 * 5120 + (w - 5) * 1024));
    BSTORE16((v4u{jf[2][0], jf[2][1], jf[3][0], jf[3][1]}), (f1 < 10 ? k0 < 22 : (k0 < 20 && w < 7)) ? l16 : OOB, d0 + (f1 < 10 ? 32 * 5120 + (f1 - 5) * 1024 : 54 * 5120 + (f1 - 10) * 1024));
    WAITV(0);
    RAWBAR();
    if (tid < 2) set_flag(p.ws, tid == 0 ? WS_F2 + (rt64 * 8 + ct) * 128 : WS_FW + b * 8, tag);
    STAMP(6);
  }
  sfor<7>([&](auto I) { w3load(IC<I + 4>{}); });
  sfor<7>([&](auto I) { constexpr int n = 11 + I; dsw16<(n - 11) * 8192>(l16 + 53 * 1024 + (n == 17 ? (w & 3) * 1024 : lw), w3s[I]); });
  {
    if (w == 6) {
      const unsigned co = WS_CS + (640 + (6 * cg + (cg < 12 ? cg : 12)) * 16) * 8, cv_ = (lane < (cg < 12 ? 56 : 48) ? lane : 0) * 16;
      v4u c;
#pragma unroll 1
      for (unsigned spins = 0; spins < (1u << 16); ++spins) {
        c = __builtin_amdgcn_raw_buffer_load_b128(rs, cv_, co, 16);
        if (__all(c[1] == tag32 && c[3] == tag32)) break;
      }
      *(LAS v2u*)(L + L_CS3 + lane * 8) = v2u{c[0], c[2]};
    }
    if (w == 7) poll_flags(p.ws, WS_F2 + (rt128 * 16 + r) * 128, tag);
    WAITV(0);
    BAR();
    STAMP(8);
    const int rt = rt128 * 8 + w;
    constexpr int PD = 10;
    bf16x8 af[20];
    const char* ap = p.ws + WS_X1 + (size_t)rt * 20 * 1024;
    sfor<PD>([&](auto I) { constexpr int i = I; gload16<(i & 3) * 1024>(af[i], ap + (i >> 2) * 4096, l16); });
    f32x4 acc3[7]; f32x2 stp[4];
    bf16x8 bfr[7];
    const unsigned c1 = (4 * g + q4) * 224 + pp * 8, c0 = c1 + 122880, c2 = c1 + 57344;
    sfor<7>([&](auto T) { constexpr int t = T; trb<t, t * 32, t * 32 + 3584>(bfr[t], c0); });
    u64 fwv, fqv;
    const unsigned fwoff = WS_FW + (w * 32 + (lane & 31)) * 8;
    unsigned fqoff;
    {
      const int own = rt64 >> 1, pv = ((rt64 & 1) || own == 0) ? own : own - 1;
      fqoff = WS_FQ + (lane < 10 ? own * 128 + lane * 8 : lane < 20 ? pv * 128 + (lane - 10) * 8 : own * 128);
    }
    const unsigned qso = WS_QKV + ((rt128 * 4 + (w & 1) * 2) * 120 + (5 * half + sidx) * 12 + (w >> 1) * 3) * 1024;
    int art = 2 * rt64 + (w < 2 ? w : w < 5 ? w - 3 : w - 6); if (art < 0) art = 0;
    const char* asb = p.ws + WS_QKV + (size_t)(art * 120 + (w < 2 ? 0 : w < 5 ? 40 : 80) + 5 * ct) * 1024;
    sfor<20>([&](auto I) {
      constexpr int ks = I;
      constexpr auto nA = [](int s) { return s <= 17 ? 1 : 0; };
      constexpr auto nB = [](int s) { return (s == 7 || s >= 9) ? 1 : 0; };
      constexpr int cnt = [&]() {
        int n = 0;
        if (ks < PD) { n = PD - 1 - ks; for (int s = 0; s < ks; ++s) n += nA(s) + nB(s); }
        else { int s0 = ks - PD; n = nB(s0); for (int s = s0 + 1; s < ks; ++s) n += nA(s) + nB(s); }
        return n; }();
      if constexpr (ks == 1 || ks == 5) STAMP(16 + ks / 4);
      if constexpr (ks == 9) {
        STAMP(18);
        WAITV_R(1, fwv);
        if (!__all(fwv == tag)) poll_flags(p.ws, fwoff, tag);
      }
      WAITV_R(cnt, af[ks]);
      if constexpr (ks == 9 || ks == 12 || ks == 15) RAWBAR();
      if constexpr (ks == 9) STAMP(19);
      if constexpr (ks == 15) STAMP(21);
      if constexpr (ks == 9) { if (tid == 0 && role == 0) set_flag(p.ws, WS_FQ + rt128 * 128 + (5 * half + sidx) * 8, tag); }
      sfor<7>([&](auto T) {
        constexpr int t = T;
        if constexpr (ks < 19) WAITL(12); else WAITL(12 - 2 * t);
        mmb<t, ks == 0>(acc3[t], bfr[t], af[ks]);
        if constexpr (ks < 19) { constexpr int k1 = ks + 1, o = (k1 < 5 ? k1 : k1 < 13 ? k1 - 5 : k1 - 13) * 7168 + t * 32; trb<t, o, o + 3584>(bfr[t], k1 < 5 ? c0 : k1 < 13 ? c1 : c2); }
        if constexpr (t == 1) {
          if constexpr (ks < 10) gload16<((ks + PD) & 3) * 1024>(af[ks + PD], ap + ((ks + PD) >> 2) * 4096, l16);
          else if constexpr (ks < 14) gload8<(ks - 10) * 128>(stp[ks - 10], p.ws + WS_ST + (size_t)rt * 2048, (unsigned)(g * 512 + r * 8));
          else if constexpr (ks < 17) { constexpr int f = 40 + 16 * (ks - 14); bload<f, B0 + f>(sb4, l16, lw); }
          else if constexpr (ks == 17) gload8<0>(fqv, p.ws, fqoff);
        }
        if constexpr (t == 4) {
          if constexpr (ks == 7) gload8<0>(fwv, p.ws, fwoff);
          else if constexpr (ks >= 9) {
            constexpr int fs[11] = {0, 8, 16, 24, 32, 48, 64, 80, 88, 96, 102};
            bload<fs[ks - 9], B0 + fs[ks - 9]>(sb4, l16, lw);
          }
        }
        if constexpr (t == 6 && ks < 6) { if (role == 0) BSTORE16(qp[ks], l16, qso + (ks >> 1) * 1024 + (ks & 1) * 120 * 1024); }
      });
    });
    STAMP(20);
    WAITV_R(3, fqv);
    if (!__all(fqv == tag)) poll_flags(p.ws, fqoff, tag);
    f32x4 cv[7];
    const unsigned cva = L_CS3 + g * 16;
    sfor<7>([&](auto T) { constexpr int t = T; dsr16<t * 64>(cv[t], cva); });
    asm volatile("s_barrier\n\ts_nop 7" : "+v"(acc3[0]), "+v"(acc3[1]), "+v"(acc3[2]), "+v"(acc3[3]), "+v"(acc3[4]), "+v"(acc3[5]), "+v"(acc3[6]), "+v"(stp[0]), "+v"(stp[1]), "+v"(stp[2]), "+v"(stp[3])::"memory");
    STAMP(9);
    glds(asb, l16, L_ATT + w * 5120); glds_same_m0<1024>(asb, l16); glds_same_m0<2048>(asb, l16); glds_same_m0<3072>(asb, l16);
    glds(asb + 4096, l16, L_ATT + w * 5120 + 4096);
    const float sum = xsum4((stp[0][0] + stp[1][0]) + (stp[2][0] + stp[3][0]));
    const float sq = xsum4((stp[0][1] + stp[1][1]) + (stp[2][1] + stp[3][1]));
    const float mean = sum * (1.0f / D);
    const float var = fmaxf(sq * (1.0f / D) - mean * mean, 0.f) + 1e-5f;
    const float rstd = __builtin_amdgcn_rsqf(var), sd = var * rstd;
    const float k1 = GC1 * rstd, k2 = GC2 * rstd * rstd * rstd;
    asm volatile("s_waitcnt lgkmcnt(0)" : "+v"(cv[0]), "+v"(cv[1]), "+v"(cv[2]), "+v"(cv[3]), "+v"(cv[4]), "+v"(cv[5]), "+v"(cv[6]));
    const unsigned so = WS_U + rt * 54 * 1024;
    sfor<4>([&](auto PR) {
      constexpr int pr = PR;
      v4u o;
#pragma unroll
      for (int tt = 0; tt < (pr < 3 ? 2 : 1); ++tt) {
        const int t = 2 * pr + tt;
        float h[4];
#pragma unroll
        for (int e = 0; e < 4; ++e) h[e] = gelu_s(__builtin_fmaf(-mean, cv[t][e], acc3[t][e]), k1, k2, sd);
        o[2 * tt] = pk2(h[0], h[1]); o[2 * tt + 1] = pk2(h[2], h[3]);
      }
      if constexpr (pr < 3) BSTORE16(o, l16, so + (3 * cg + pr) * 1024);
      else {
        const v2u s0 = sw16(o[0], o[0]), s1 = sw16(o[1], o[1]);
        BSTORE16((v4u{s0[0], s1[0], s0[1], s1[1]}), (cg < 12 && !(g & 1)) ? (g >> 1) * 256 + r * 16 : OOB, so + 48 * 1024 + cg * 512);
      }
      SB();
    });
    STAMP(10);
  }
  constexpr int NSTG = 24;
  v4u stg[NSTG];
  auto stage = [&](auto Q) { constexpr int q = Q; gload16<0>(stg[q], sb4 + (size_t)(150 + 8 * q) * 1024, l16); };
  WAITV(4);
  RAWBAR();
  {
    const unsigned offc = (wm >> 1) * 5120 + (wm & 1) * 8;
    const unsigned offp = ((wm + 1) >> 1) * 5120 + ((wm + 1) & 1) * 8;
    auto qaddr = [&](int Q, int row) { return (unsigned)(L_ATT + (Q >> 2) * 1024 + ((Q & 3) * 16 + row) * 16); };
    bf16x4 ql[2], qh[2], kpl[2], kph[2], kcl[2], kch[2], vp0[2], vc0[2], vp1[2], vc1[2];
#pragma unroll
    for (int h = 0; h < 2; ++h) {
      const int hh = 2 * wk + h;
      const unsigned a0 = qaddr(5 * hh + g, r), a1 = qaddr(5 * hh + 4, r);
      const unsigned v0 = qaddr(5 * hh + pp, 4 * g + q4), v1 = qaddr(5 * hh + 4, 4 * g + q4);
      ADSR8(ql[h], a0 + offc, 0); ADSR8(qh[h], a1 + offc, 0);
      ADSR8(kcl[h], a0 + offc, 15360); ADSR8(kch[h], a1 + offc, 15360);
      ADSR8(kpl[h], a0 + offp, 10240); ADSR8(kph[h], a1 + offp, 10240);
      ADSTR(vc0[h], v0 + offc, 30720); ADSTR(vc1[h], v1 + offc, 30720);
      ADSTR(vp0[h], v0 + offp, 25600); ADSTR(vp1[h], v1 + offp, 25600);
    }
    asm volatile("s_waitcnt lgkmcnt(0)" : "+v"(ql[0]), "+v"(qh[0]), "+v"(kcl[0]), "+v"(kch[0]), "+v"(kpl[0]), "+v"(kph[0]), "+v"(vc0[0]), "+v"(vc1[0]), "+v"(vp0[0]), "+v"(vp1[0]),
                 "+v"(ql[1]), "+v"(qh[1]), "+v"(kcl[1]), "+v"(kch[1]), "+v"(kpl[1]), "+v"(kph[1]), "+v"(vc0[1]), "+v"(vc1[1]), "+v"(vp0[1]), "+v"(vp1[1])::"memory");
    const int dd = r - 4 * g;
    const bool prev_ok = (rt64 | wm) != 0;
    unsigned o0p[2][2], o1p[2][2];
#pragma unroll
    for (int h = 0; h < 2; ++h) {
      bf16x4 qhh = qh[h];
      if (g != 0) qhh = bf16x4{0, 0, 0, 0};
      const bf16x8 qf = cat(ql[h], qhh);
      const f32x4 z = {0, 0, 0, 0};
      f32x4 sp = mfma(cat(kpl[h], kph[h]), qf, z), sc = mfma(cat(kcl[h], kch[h]), qf, z);
      const float NEG = -__builtin_inff();
      float mx = NEG;
#pragma unroll
      for (int e = 0; e < 4; ++e) {
        const int dc = dd - e;
        const bool even = (dc & 1) == 0;
        const bool vc = even && dc >= 0, vp = even && dc < 0 && prev_ok;
        sp[e] = vp ? sp[e] : NEG; sc[e] = vc ? sc[e] : NEG;
        mx = fmaxf(mx, fmaxf(sp[e], sc[e]));
      }
      mx = xmax4(mx);
      const float cexp = 0.22360679774997896f * 1.4426950408889634f;
      const float mc = mx * cexp;
      float sum = 0;
#pragma unroll
      for (int e = 0; e < 4; ++e) {
        sp[e] = __builtin_amdgcn_exp2f(__builtin_fmaf(sp[e], cexp, -mc)); sc[e] = __builtin_amdgcn_exp2f(__builtin_fmaf(sc[e], cexp, -mc));
        sum += sp[e] + sc[e];
      }
      sum = xsum4(sum);
      const v4u pu = {pk2(sp[0], sp[1]), pk2(sp[2], sp[3]), pk2(sc[0], sc[1]), pk2(sc[2], sc[3])};
      const bf16x8 pf = __builtin_bit_cast(bf16x8, pu);
      const f32x4 o0 = mfma(cat(vp0[h], vc0[h]), pf, z), o1 = mfma(cat(vp1[h], vc1[h]), pf, z);
      const float inv = __builtin_amdgcn_rcpf(sum);
      o0p[h][0] = pk2(o0[0] * inv, o0[1] * inv); o0p[h][1] = pk2(o0[2] * inv, o0[3] * inv);
      o1p[h][0] = pk2(o1[0] * inv, o1[1] * inv); o1p[h][1] = pk2(o1[2] * inv, o1[3] * inv);
      if (h == 0) {
        SB();
        WAITV(0);
        RAWBAR();
        if (tid == 0) set_flag(p.ws, WS_F3 + (rt128 * 16 + cg) * 128, tag);
        STAMP(11);
        sfor<5>([&](auto I) { constexpr int f = 110 + 8 * I; bload<f, B0 + f>(sb4, l16, lw); });
        sfor<10>(stage);
        SB();
      }
    }
    const unsigned so = WS_O + (rt64 * 4 + wm) * 20 * 1024;
    BSTORE16((v4u{o0p[0][0], o0p[0][1], o0p[1][0], o0p[1][1]}), l16, so + (2 * ct + wk) * 1024);
    BSTORE16((v4u{o1p[0][0], o1p[0][1], o1p[1][0], o1p[1][1]}), g == 0 ? l16 : OOB, so + 16 * 1024 + ct * 512 + wk * 256);
  }
  STAMP(12);
  {
    if (w == 7) poll_flags(p.ws, WS_F3 + ((rt64 >> 1) * 16 + r) * 128, tag);
    sfor<14>([&](auto I) { stage(IC<10 + I>{}); });
    if (b == 0 && tid == 0) set_flag(p.ws, WS_EP, tag);
    WAITV(26);
    RAWBAR();
    STAMP(13);
    const unsigned bA0 = l16 + wk * 5120, bA1 = bA0 + 61440, bA2 = bA0 + 122880;
#define BRD(dst, s) do { if constexpr ((s) < 60) ADSR(dst, bA0, (s) * 1024); else if constexpr ((s) < 120) ADSR(dst, bA1, ((s) - 60) * 1024); else ADSR(dst, bA2, ((s) - 120) * 1024); } while (0)
    const unsigned wA0 = l16 + lw, wA1 = wA0 + 61440, wA2 = wA0 + 122880;
    constexpr int PD = 10, NS = 37;
    bf16x8 af[NS];
    f32x4 acco[5];
    float gate; u64 fav;
    const unsigned faoff = WS_FA + (rt64 * 8 + (lane & 7)) * 128;
    const char* ap = p.ws + WS_U + (size_t)((rt64 * 4 + wm) * 54 + wk) * 1024;
    const char* op = p.ws + WS_O + (size_t)((rt64 * 4 + wm) * 20 + wk) * 1024;
    sfor<PD>([&](auto I) { constexpr int i = I; gload16<(i & 1) * 2048>(af[i], ap + (i >> 1) * 4096, l16); });
    bf16x8 bfr[5];
    sfor<5>([&](auto T) { constexpr int t = T; BRD(bfr[t], B0 + t); });
    sfor<NS>([&](auto I) {
      constexpr int i = I;
      constexpr auto nA = [](int s) { return (s + PD <= NS ? 1 : 0) + (s == 12 ? 1 : 0); };
      constexpr auto gB = [](int s) { int n = s < 2 ? 0 : (s - 2) + (s - 2) / 4; return n > 28 ? 28 : n; };
      constexpr auto nG = [=](int s) { return gB(s + 1) - gB(s); };
      constexpr auto nB = [=](int s) { int a = gB(s) < NSTG ? NSTG : gB(s), c = gB(s + 1) < NSTG ? NSTG : gB(s + 1); return c - a; };
      constexpr int cntA = [&]() {
        int n = 0;
        if (i < PD) { n = PD - 1 - i; for (int s = 0; s < i; ++s) n += nA(s) + nB(s); }
        else { int s0 = i - PD; n = nB(s0) + (s0 == 12 ? 1 : 0); for (int s = s0 + 1; s < i; ++s) n += nA(s) + nB(s); }
        return n; }();
      constexpr int cntB = [&]() {
        if (10 * (i + 3) <= 150 + 8 * NSTG) return 63;
        int q = (10 * (i + 3) - 150 + 7) / 8 - 1; if (q > 27) q = 27;
        int s0 = 2; while (gB(s0 + 1) <= q) ++s0;
        int n = 0;
        for (int s = s0 + 1; s < i; ++s) n += nA(s) + nB(s);
        return n; }();
      constexpr bool bar = (i & 1) == 0 && i >= 2 && i <= 34;
      if constexpr (i == 17) {
        constexpr int c = nB(12) + nA(13) + nB(13) + nA(14) + nB(14) + nA(15) + nB(15) + nA(16) + nB(16);
        WAITV_R(c, fav);
        if (!__all(fav == tag)) poll_flags(p.ws, faoff, tag);
      }
      WAITV_R((bar && cntB < cntA) ? cntB : cntA, af[i]);
      if constexpr (bar) RAWBAR();
      if constexpr (i == 2 || i == 10 || i == 18 || i == 26) STAMP(22 + i / 8);
      if constexpr (i == 2) { if (tid == 0) set_flag(p.ws, WS_FA + (rt64 * 8 + ct) * 128, tag); }
      sfor<5>([&](auto T) {
        constexpr int t = T;
        if constexpr (i < NS - 1) WAITL(4); else WAITL(4 - t);
        if constexpr (i == 0) AMFMA0(acc[t], bfr[t], af[i]); else if constexpr (i < 27) AMFMA(acc[t], bfr[t], af[i]);
        else if constexpr (i == 27) AMFMA0(acco[t], bfr[t], af[i]); else AMFMA(acco[t], bfr[t], af[i]);
        if constexpr (i < NS - 1) BRD(bfr[t], (B0 + 10 * (i + 1)) % RING + t);
        if constexpr (t == 0) {
          constexpr int n = i + PD;
          if constexpr (n < 27) gload16<(n & 1) * 2048>(af[n], ap + (n >> 1) * 4096, l16);
          else if constexpr (n < NS) gload16<((n - 27) & 1) * 2048>(af[n], op + ((n - 27) >> 1) * 4096, l16);
          else if constexpr (n == NS) gload4<0>(gate, p.ws + WS_GT + (rt64 * 64 + wm * 16) * 16, (unsigned)(r * 16));
        }
        if constexpr (t == 1 && i == 12) gload8<0>(fav, p.ws, faoff);
        if constexpr ((t == 2 && nG(i) >= 1) || (t == 4 && nG(i) == 2)) {
          constexpr int q = gB(i) + (t == 4 ? 1 : 0); constexpr int f = q < 27 ? 150 + 8 * q : 362;
          if constexpr (q < NSTG) {
            constexpr int sl = (B0 + f) % RING;
            if constexpr (sl + 7 < RING) dsw16<(sl % 60) * 1024>(sl < 60 ? wA0 : sl < 120 ? wA1 : wA2, stg[q]);
            else dsw16<0>(l16 + (sl + w >= RING ? sl + w - RING : sl + w) * 1024, stg[q]);
          } else bload<f, B0 + f>(sb4, l16, lw);
        }
      });
    });
    WAITV_R(0, gate);
    MFMA_FENCE5(acc);
    MFMA_FENCE5(acco);
    STAMP(14);
    const float ng = 1.f - gate;
#pragma unroll
    for (int t = 0; t < 5; ++t) acc[t] = gate * acco[t] + ng * acc[t];
    reduce_wk(L + 90 * 1024, wm, wk, lane, acc);
    unsigned o_[3][2];
#pragma unroll
    for (int i = 0; i < 3; ++i) {
      o_[i][0] = pk2(__builtin_fmaf(ng, bflo(x1p[i][0]), acc[i][0]), __builtin_fmaf(ng, bfhi(x1p[i][0]), acc[i][1]));
      o_[i][1] = pk2(__builtin_fmaf(ng, bflo(x1p[i][1]), acc[i][2]), __builtin_fmaf(ng, bfhi(x1p[i][1]), acc[i][3]));
    }
    const __amdgpu_buffer_rsrc_t ro = __builtin_amdgcn_make_buffer_rsrc((void*)p.out, 0, S * D * 2, 0x00020000);
    const v2u a0 = sw16(o_[0][0], o_[1][0]), a1 = sw16(o_[0][1], o_[1][1]), s0 = sw16(o_[2][0], o_[2][0]), s1 = sw16(o_[2][1], o_[2][1]);
    __builtin_amdgcn_raw_buffer_store_b128(v4u{a0[0], a1[0], a0[1], a1[1]}, ro, opair, 0, 16);
    __builtin_amdgcn_raw_buffer_store_b128(v4u{s0[0], s1[0], s0[1], s1[1]}, ro, (wk == 0 && !(g & 1)) ? orow + 128 + (g >> 1) * 16 : OOB, 0, 16);
    STAMP(15);
  }
}

extern "C" void kernel_launch(void* const* d_in, const int* in_sizes, int n_in, void* d_out, int out_size, void* d_ws, size_t ws_size, hipStream_t stream) {
  Params p;
  p.x = (const u16*)d_in[0]; p.wqkv = (const u16*)d_in[1]; p.wo = (const u16*)d_in[2];
  p.w1 = (const u16*)d_in[4]; p.w2 = (const u16*)d_in[5]; p.w3 = (const u16*)d_in[6]; p.w4 = (const u16*)d_in[7]; p.gw = (const u16*)d_in[8];
  p.out = (u16*)d_out; p.ws = (char*)d_ws;
  const int lds = 163840;
  (void)hipFuncSetAttribute((const void*)mega, hipFuncAttributeMaxDynamicSharedMemorySize, lds);
  mega<<<256, 512, lds, stream>>>(p);
}
```

```cpp
#include <hip/hip_runtime.h>
#include <stdint.h>
#include <type_traits>

typedef unsigned short u16;
typedef unsigned long long u64;
typedef short bf16x8 __attribute__((ext_vector_type(8)));
typedef short bf16x4 __attribute__((ext_vector_type(4)));
typedef float f32x4 __attribute__((ext_vector_type(4)));
typedef float f32x2 __attribute__((ext_vector_type(2)));
typedef __bf16 bfv2 __attribute__((ext_vector_type(2)));
typedef unsigned v4u __attribute__((ext_vector_type(4)));
typedef unsigned v2u __attribute__((ext_vector_type(2)));
#define LAS __attribute__((address_space(3)))
#define GAS __attribute__((address_space(1)))
#define DEV __device__ __forceinline__
#define RLX_AGENT __ATOMIC_RELAXED, __HIP_MEMORY_SCOPE_AGENT

constexpr int S = 2048, D = 640, F = 1728;
constexpr unsigned KB = 1024;
constexpr unsigned WS_W4 = 0;
constexpr unsigned WS_QKV = WS_W4 + 2960 * KB;
constexpr unsigned WS_HS = WS_QKV + 7680 * KB;
constexpr unsigned WS_O = WS_HS + 2560 * KB;
constexpr unsigned WS_X1 = WS_O + 2560 * KB;
constexpr unsigned WS_U = WS_X1 + 2560 * KB;
constexpr unsigned WS_ST = WS_U + 6912 * KB;
constexpr unsigned WS_RS = WS_ST + 256 * KB;
constexpr unsigned WS_GT = WS_RS + 32 * KB;
constexpr unsigned WS_CS = WS_GT + 32 * KB;
constexpr unsigned WS_EP = WS_CS + 20 * KB;
constexpr unsigned WS_FW = WS_EP + 1 * KB;
constexpr unsigned WS_FQ = WS_FW + 2 * KB;
constexpr unsigned WS_FH = WS_FQ + 2 * KB;
constexpr unsigned WS_F2 = WS_FH + 16 * KB;
constexpr unsigned WS_FA = WS_F2 + 32 * KB;
constexpr unsigned WS_F3 = WS_FA + 32 * KB;
constexpr unsigned WS_END = WS_F3 + 32 * KB;
constexpr unsigned OOB = 0x40000000u;
constexpr unsigned L_JOB = 104 * KB, L_ATT = 70 * KB, L_CS3 = 156 * KB;
constexpr int B0 = 110;
constexpr int RING = 150;
#define QD 8
constexpr unsigned CODE_LINES = 284;

DEV f32x4 mfma(bf16x8 a, bf16x8 b, f32x4 c) { return __builtin_amdgcn_mfma_f32_16x16x32_bf16(a, b, c, 0, 0, 0); }
DEV unsigned pk2(float a, float b) { f32x2 v = {a, b}; bfv2 r = __builtin_convertvector(v, bfv2); return __builtin_bit_cast(unsigned, r); }
DEV float bflo(unsigned d) { return __uint_as_float(d << 16); }
DEV float bfhi(unsigned d) { return __uint_as_float(d & 0xffff0000u); }
DEV float dot2(unsigned a, unsigned b, float c) { return __builtin_amdgcn_fdot2_f32_bf16(__builtin_bit_cast(bfv2, a), __builtin_bit_cast(bfv2, b), c, false); }
DEV float gelu_s(float a, float k1, float k2, float sd) {
  float t = a * __builtin_fmaf(a * a, k2, k1);
  float e = __builtin_amdgcn_exp2f(t);
  return a * __builtin_amdgcn_rcpf(__builtin_fmaf(e, sd, sd));
}
constexpr float GC1 = -2.0f * 0.7978845608028654f * 1.4426950408889634f, GC2 = GC1 * 0.044715f;
DEV void glds(const void* sbase, unsigned voff, unsigned lds) {
  asm volatile("s_mov_b32 m0, %2\n\ts_nop 0\n\tglobal_load_lds_dwordx4 %0, %1 sc1" ::"v"(voff), "s"(sbase), "s"(lds) : "memory");
}
template <int IMM> DEV void glds_same_m0(const void* sbase, unsigned voff) {
  asm volatile("global_load_lds_dwordx4 %0, %1 offset:%2 sc1" ::"v"(voff), "s"(sbase), "n"(IMM) : "memory");
}
template <int IMM, typename T> DEV void gload16(T& d, const void* sbase, unsigned voff) {
  asm volatile("global_load_dwordx4 %0, %1, %2 offset:%3 sc1" : "=v"(d) : "v"(voff), "s"(sbase), "n"(IMM) : "memory");
}
template <int IMM, typename T> DEV void gload8(T& d, const void* sbase, unsigned voff) {
  asm volatile("global_load_dwordx2 %0, %1, %2 offset:%3 sc1" : "=v"(d) : "v"(voff), "s"(sbase), "n"(IMM) : "memory");
}
template <int IMM, typename T> DEV void gload4(T& d, const void* sbase, unsigned voff) {
  asm volatile("global_load_dword %0, %1, %2 offset:%3 sc1" : "=v"(d) : "v"(voff), "s"(sbase), "n"(IMM) : "memory");
}
DEV bf16x8 cat(bf16x4 a, bf16x4 b) { return __builtin_shufflevector(a, b, 0, 1, 2, 3, 4, 5, 6, 7); }
#define WAITV(n) asm volatile("s_waitcnt vmcnt(%0)" ::"n"(n) : "memory")
#define WAITV_R(n, r) asm volatile("s_waitcnt vmcnt(%1)" : "+v"(r) : "n"(n) : "memory")
#define PIN(r) asm volatile("" : "+v"(r))
#define BAR() asm volatile("s_waitcnt lgkmcnt(0)\n\ts_barrier" ::: "memory")
#define RAWBAR() asm volatile("s_barrier" ::: "memory")
#define SB() __builtin_amdgcn_sched_barrier(0)
#define AMFMA(acc, a, b) asm volatile("v_mfma_f32_16x16x32_bf16 %0, %1, %2, %0" : "+v"(acc) : "v"(a), "v"(b))
#define AMFMA0(acc, a, b) asm volatile("v_mfma_f32_16x16x32_bf16 %0, %1, %2, 0" : "=&v"(acc) : "v"(a), "v"(b))
#define ADSR(dst, addr, off) asm volatile("ds_read_b128 %0, %1 offset:%2" : "=v"(dst) : "v"(addr), "n"(off))
#define ADSR8(dst, addr, off) asm volatile("ds_read_b64 %0, %1 offset:%2" : "=v"(dst) : "v"(addr), "n"(off))
#define ADSTR(dst, addr, off) asm volatile("ds_read_b64_tr_b16 %0, %1 offset:%2" : "=v"(dst) : "v"(addr), "n"(off))
#define WAITL(n) asm volatile("s_waitcnt lgkmcnt(%0)" ::"n"(n))
#define TRW(var, R0, R1, R2, R3, addr, off) asm volatile("ds_read_b64_tr_b16 v[" #R0 ":" #R1 "], %1 offset:%2\n\tds_read_b64_tr_b16 v[" #R2 ":" #R3 "], %1 offset:%3" : "={v[" #R0 ":" #R3 "]}"(var) : "v"(addr), "n"(off), "n"((off) + 512))
#define MMW(first, acc, var, R0, R3, x) do { if constexpr (first) asm volatile("v_mfma_f32_16x16x32_bf16 %0, %1, %2, 0" : "=&v"(acc) : "{v[" #R0 ":" #R3 "]}"(var), "v"(x)); \
    else asm volatile("v_mfma_f32_16x16x32_bf16 %0, %1, %2, %0" : "+v"(acc) : "{v[" #R0 ":" #R3 "]}"(var), "v"(x)); } while (0)
#define TRP(R0, R1, R2, R3) asm volatile("ds_read_b64_tr_b16 v[" #R0 ":" #R1 "], %1 offset:%2\n\tds_read_b64_tr_b16 v[" #R2 ":" #R3 "], %1 offset:%3" : "={v[" #R0 ":" #R3 "]}"(d) : "v"(a), "n"(O1), "n"(O2))
template <int T, int O1, int O2> DEV void trb(bf16x8& d, unsigned a) {
  if constexpr (T == 0) TRP(100, 101, 102, 103); else if constexpr (T == 1) TRP(104, 105, 106, 107); else if constexpr (T == 2) TRP(108, 109, 110, 111); else if constexpr (T == 3) TRP(112, 113, 114, 115);
  else if constexpr (T == 4) TRP(116, 117, 118, 119); else if constexpr (T == 5) TRP(120, 121, 122, 123); else TRP(124, 125, 126, 127);
}
template <int T, bool FI> DEV void mmb(f32x4& acc, bf16x8& wv, bf16x8& x) {
  if constexpr (T == 0) MMW(FI, acc, wv, 100, 103, x); else if constexpr (T == 1) MMW(FI, acc, wv, 104, 107, x); else if constexpr (T == 2) MMW(FI, acc, wv, 108, 111, x); else if constexpr (T == 3) MMW(FI, acc, wv, 112, 115, x);
  else if constexpr (T == 4) MMW(FI, acc, wv, 116, 119, x); else if constexpr (T == 5) MMW(FI, acc, wv, 120, 123, x); else MMW(FI, acc, wv, 124, 127, x);
}
template <typename T> DEV void pin(T& v) { PIN(v); }
template <int OFF, typename T> DEV void dstr(T& d, unsigned a) { ADSTR(d, a, OFF); }
template <int OFF> DEV void dsw16(unsigned a, v4u v) { asm volatile("ds_write_b128 %0, %1 offset:%2" ::"v"(a), "v"(v), "n"(OFF) : "memory"); }
template <int OFF, typename T> DEV void dsr16(T& d, unsigned a) { ADSR(d, a, OFF); }
DEV void bstore16(v4u v, v4u rq, unsigned voff, unsigned soff) {
  asm volatile("buffer_store_dwordx4 %0, %1, %2, %3 offen sc1\n\ts_nop 1" ::"v"(v), "v"(voff), "s"(rq), "s"(soff) : "memory");
}
#define BSTORE16(v, voff, soff) bstore16(v, rq, voff, soff)

struct Params {
  const u16 *x, *wqkv, *wo, *w1, *w2, *w3, *w4, *gw;
  u16* out;
  char* ws;
};

template <int V> using IC = std::integral_constant<int, V>;
template <int N, typename Fn> DEV void sfor_impl(Fn&& f) { if constexpr (N > 0) { sfor_impl<N - 1>(f); f(IC<N - 1>{}); } }
template <int N, typename Fn> DEV void sfor(Fn&& f) { sfor_impl<N>(f); }

DEV float xsum4(float v) {
  unsigned u = __float_as_uint(v);
  auto a = __builtin_amdgcn_permlane16_swap(u, u, false, false);
  v = __uint_as_float(a[0]) + __uint_as_float(a[1]);
  u = __float_as_uint(v);
  auto b = __builtin_amdgcn_permlane32_swap(u, u, false, false);
  return __uint_as_float(b[0]) + __uint_as_float(b[1]);
}
DEV float xmax4(float v) {
  unsigned u = __float_as_uint(v);
  auto a = __builtin_amdgcn_permlane16_swap(u, u, false, false);
  v = fmaxf(__uint_as_float(a[0]), __uint_as_float(a[1]));
  u = __float_as_uint(v);
  auto b = __builtin_amdgcn_permlane32_swap(u, u, false, false);
  return fmaxf(__uint_as_float(b[0]), __uint_as_float(b[1]));
}
DEV v2u sw16(unsigned a, unsigned b) { auto t = __builtin_amdgcn_permlane16_swap(a, b, false, false); return v2u{t[0], t[1]}; }
DEV unsigned hi32(unsigned u) { auto b = __builtin_amdgcn_permlane32_swap(u, u, false, false); return b[1]; }
template <int CTRL> DEV float dpp_add(float v) { return v + __uint_as_float(__builtin_amdgcn_update_dpp(0, __float_as_uint(v), CTRL, 0xf, 0xf, true)); }
DEV float sum32(float v) {
  v = dpp_add<0xB1>(v); v = dpp_add<0x4E>(v); v = dpp_add<0x141>(v); v = dpp_add<0x140>(v);
  unsigned u = __float_as_uint(v);
  auto a = __builtin_amdgcn_permlane16_swap(u, u, false, false);
  return __uint_as_float(a[0]) + __uint_as_float(a[1]);
}
DEV float sum8(float v) { v = dpp_add<0xB1>(v); v = dpp_add<0x4E>(v); return dpp_add<0x141>(v); }

DEV void poll_flags(const char* ws, unsigned off, u64 tag) {
  GAS u64* f = (GAS u64*)(ws + off);
#pragma unroll 1
  for (unsigned spins = 0; spins < (1u << 16); ++spins) {
    u64 v = __hip_atomic_load(f, RLX_AGENT);
    if (__all(v == tag)) break;
    __builtin_amdgcn_s_sleep(1);
  }
}
DEV void set_flag(char* ws, unsigned off, u64 tag) { __hip_atomic_store((GAS u64*)(ws + off), tag, RLX_AGENT); }

DEV void reduce_wk(LAS char* redb, int wm, int wk, int lane, f32x4 (&acc)[5]) {
  LAS f32x4* red = (LAS f32x4*)redb + wm * 5 * 64 + lane;
  if (wk == 0) { red[2 * 64] = acc[2]; red[3 * 64] = acc[3]; }
  else { red[0] = acc[0]; red[64] = acc[1]; red[4 * 64] = acc[4]; }
  BAR();
  if (wk == 0) { acc[0] += red[0]; acc[1] += red[64]; acc[2] = acc[4] + red[4 * 64]; }
  else { acc[0] = acc[2] + red[2 * 64]; acc[1] = acc[3] + red[3 * 64]; acc[2] = f32x4{0, 0, 0, 0}; }
}
#define MFMA_FENCE5(a) asm volatile("s_nop 7\n\ts_nop 1" : "+v"(a[0]), "+v"(a[1]), "+v"(a[2]), "+v"(a[3]), "+v"(a[4]))

template <int FRAG, int POS> DEV void bload(const char* sbw, unsigned l16, unsigned lw) {
  constexpr int sl = POS % RING;
  if constexpr (sl + 7 < RING) glds(sbw + (size_t)FRAG * 1024, l16, lw + sl * 1024u);
  else { unsigned d = lw + sl * 1024u; if (d >= RING * 1024u) d -= RING * 1024u; glds(sbw + (size_t)FRAG * 1024, l16, d); }
}

__global__ __launch_bounds__(512, 2) void mega(Params p) {
  extern __shared__ __attribute__((aligned(16))) char smem[];
  LAS char* L = (LAS char*)smem;
  u64 pc0;
  asm volatile("s_getpc_b64 %0" : "=s"(pc0));
  pc0 &= ~255ull;
  const int b = blockIdx.x, tid = threadIdx.x, lane = tid & 63;
  const int w = __builtin_amdgcn_readfirstlane(tid >> 6);
  const int xcd = b & 7, j = b >> 3, grp = xcd >> 1, half = xcd & 1;
  const int rt128 = 4 * grp + (j & 3), sidx = j >> 2, cg = 8 * half + sidx;
  const int rt64 = 8 * grp + (j & 7), ct = 4 * half + (j >> 3);
  const int wm = w & 3, wk = w >> 2, r = lane & 15, g = lane >> 4;
  const unsigned l16 = lane * 16, lw = w * 1024;
  const __amdgpu_buffer_rsrc_t rs = __builtin_amdgcn_make_buffer_rsrc((void*)p.ws, 0, WS_END, 0x00020000);
  const v4u rq = {(unsigned)(size_t)p.ws, (unsigned)((size_t)p.ws >> 32) & 0xffffu, WS_END, 0x00020000u};
  const char* sb2 = (const char*)p.w2 + ct * 160;
  unsigned v2[5], v3[7], v3c[4];
  const int cmax3 = cg < 12 ? 13 : 11;
  auto w2init = [&]() {
#pragma unroll
  for (int k = 0; k < 7; ++k) {
    const unsigned u = (8 * k + w) * 64 + lane, row = (u * 4682) >> 16; unsigned ch = u - row * 14; if (ch > cmax3) ch = cmax3;
    v3[k] = (row + (row >> 6) * 16) * 3456 + ch * 16; PIN(v3[k]);
  }
#pragma unroll
  for (int k = 0; k < 4; ++k) {
    const unsigned u = (8 * k + (k == 3 ? (w & 3) : w)) * 64 + lane, row = (u * 4682) >> 16; unsigned ch = u - row * 14; if (ch > cmax3) ch = cmax3;
    v3c[k] = (160 * (row >> 5) + 64 + 80 * ((row >> 3) & 1) + 8 * ((row >> 2) & 1) + 4 * ((row >> 4) & 1) + (row & 3)) * 3456 + ch * 16; PIN(v3c[k]);
  }
#pragma unroll
    for (int k = 0; k < 5; ++k) { const unsigned u = (8 * k + w) * 64 + lane, row = (u * 6554) >> 16; v2[k] = row * 1120 + u * 16; PIN(v2[k]); }
  };
  auto w2load = [&](auto N) {
    constexpr int n = N;
    if constexpr (n < 12) glds(sb2 + (size_t)(n / 5) * 256 * 1280, v2[n % 5], lw + n * 8192);
    else glds(sb2 + (size_t)(w < 4 ? 512 : 256) * 1280, v2[2], lw + n * 8192);
  };
  v4u qp[6];
  u64 tag; unsigned tag32;
  v2u ep;
  auto make_tag = [&]() {
    unsigned e0, e1;
    asm volatile("v_readfirstlane_b32 %0, %2\n\tv_readfirstlane_b32 %1, %3\n\ts_nop 3" : "=s"(e0), "=s"(e1) : "v"(ep[0]), "v"(ep[1]));
    const u64 e = ((u64)e1 << 32) | e0;
    tag = e * 6364136223846793005ull + 1442695040888963407ull; tag32 = (unsigned)(tag >> 32);
  };
  auto epload = [&]() {
    unsigned ln = (j * 8 + w) * 2 + (lane & 1); if (ln >= CODE_LINES) ln = CODE_LINES - 1;
    const u64 a = (lane == 1 || lane == 2) ? pc0 + ln * 128 : (u64)(p.ws + WS_EP);
    asm volatile("global_load_dwordx2 %0, %1, off sc1" : "=v"(ep) : "v"(a) : "memory");
  };
  const int role = sidx < 5 ? 0 : cg != 15 ? 1 : 2;
  unsigned fhoff;
  if (__builtin_expect(role != 2, 1)) {
    const bool isq = role == 0;
    const int wm2 = w & 1, wn = w >> 1;
    const int col = isq ? 5 * half + sidx : 3 * half + sidx - 5;
    const int pitch = isq ? 3840 : 1280;
    const char* sX[5];
    sX[0] = (const char*)p.x + (size_t)rt128 * 128 * 1280; sX[1] = sX[0] + 64 * 1280;
    sX[2] = (const char*)(isq ? p.wqkv : p.w1) + col * (isq ? 384 : 256); sX[3] = sX[2] + 128; sX[4] = sX[2] + 256;
    int kc = sidx + ((5 * (rt128 & 3) + 1) >> 1); if (kc >= 10) kc -= 10;
    unsigned va, vb;
    {
      const int srow = lane >> 3, ss = lane & 7, r0 = 8 * w + srow;
      va = r0 * 1280 + ((ss ^ ((r0 >> 1) & 7)) << 4) + kc * 128;
      const int swz = ((r0 >> 1) & 1) | (((r0 >> 3) & 1) << 1);
      vb = (r0 + kc * 64) * pitch + (((((ss >> 1) ^ swz) << 1) | (ss & 1)) << 4);
    }
    auto advance = [&]() {
      va += kc == 9 ? -9 * 128 : 128; vb += (kc == 9 ? -9 * 64 : 64) * pitch;
      kc = kc == 9 ? 0 : kc + 1;
    };
    epload();
    v4u sx[3], sg[3];
    if (isq) {
      const unsigned vg = (lane & 31) * 16, vs = ((col & 7) * 16 + 2 * w + (lane >> 5)) * 1280 + vg;
      gload16<0>(sx[0], sX[0], vs); gload16<512>(sx[1], sX[0], vs); gload16<1024>(sx[2], sX[0], vs - (lane & 16) * 16);
      gload16<0>(sg[0], p.gw, vg); gload16<512>(sg[1], p.gw, vg); gload16<1024>(sg[2], p.gw, (unsigned)(r * 16));
    }
    sfor<2>([&](auto C) {
      constexpr int c = C; constexpr unsigned sl = ((c + 2) % 3) * 40960;
      sfor<4>([&](auto K) { constexpr int k = K; glds(sX[k], k < 2 ? va : vb, lw + sl + k * 8192); });
      if (isq) glds(sX[4], vb, lw + sl + 4 * 8192);
      advance();
    });
    {
      const int own = rt64 >> 1, nb = (own + ((rt64 & 1) ? 1 : 15)) & 15;
      const int li = lane < 10 ? lane : 0;
      fhoff = WS_FH + (li < 5 ? own * 8 + li : nb * 8 + li - 5) * 128;
    }
    auto s1run = [&](auto NT_) {
      constexpr int NT = NT_, NG = 2 + NT; constexpr bool ISQ = NT == 3;
      unsigned mslot = 40960 + lw;
      auto issue = [&](auto K) {
        constexpr int k = K;
        if constexpr (k >= 100) w2load(IC<k - 100>{});
        else {
          glds(sX[k], k < 2 ? va : vb, mslot + k * 8192);
          if constexpr (k == NG - 1) { advance(); mslot += 40960; if (mslot >= 122880) mslot -= 122880; }
        }
      };
      sfor<NG>(issue);
      if constexpr (!ISQ) w2init();
      unsigned xo[2], wo_[NT];
      xo[0] = 81920 + wm2 * 8192 + r * 128 + ((g ^ (r >> 1)) << 4);
      xo[1] = xo[0] ^ 64;
      {
        const int q = (lane >> 2) & 3, pp = lane & 3, swz = ((q >> 1) & 1) | ((g & 1) << 1);
#pragma unroll
        for (int t = 0; t < NT; ++t) { const int U = NT * wn + t; wo_[t] = 81920 + 16384 + (U >> 2) * 8192 + (8 * g + q) * 128 + (((U & 3) ^ swz) << 5) + pp * 8; PIN(wo_[t]); }
      }
      PIN(xo[0]); PIN(xo[1]); PIN(va); PIN(vb); PIN(fhoff);
      SB();
      if constexpr (ISQ) {
        WAITV(15);
        make_tag();
        PIN(sx[0]); PIN(sx[1]); PIN(sx[2]); PIN(sg[0]); PIN(sg[1]); PIN(sg[2]);
        float sm = 0, sq = 0, gt = 0, sm2 = 0, sq2 = 0, gt2 = 0;
#pragma unroll
        for (int e = 0; e < 4; ++e) {
          sm = dot2(sx[0][e], 0x3F803F80u, sm); sq = dot2(sx[0][e], sx[0][e], sq); gt = dot2(sx[0][e], sg[0][e], gt);
          sm2 = dot2(sx[2][e], 0x3F803F80u, sm2); sq2 = dot2(sx[2][e], sx[2][e], sq2); gt2 = dot2(sx[2][e], sg[2][e], gt2);
          sm = dot2(sx[1][e], 0x3F803F80u, sm); sq = dot2(sx[1][e], sx[1][e], sq); gt = dot2(sx[1][e], sg[1][e], gt);
        }
        sm = sum32(__builtin_fmaf(sm2, 0.5f, sm)); sq = sum32(__builtin_fmaf(sq2, 0.5f, sq)); gt = sum32(__builtin_fmaf(gt2, 0.5f, gt));
        const float mean = sm * (1.0f / D);
        const float rstd = __builtin_amdgcn_rsqf(fmaxf(sq * (1.0f / D) - mean * mean, 0.0f) + 1e-5f);
        const float gate = __builtin_amdgcn_rcpf(1.0f + __expf(-gt));
        BSTORE16((v4u{__float_as_uint((lane & 1) ? gate : mean), tag32, __float_as_uint(rstd), tag32}), ((lane & 30) == 0 && col < 8) ? (lane >> 5) * 16 + (lane & 1) * (WS_GT - WS_RS) : OOB,
                 WS_RS + (rt128 * 128 + (col & 7) * 16 + 2 * w) * 16);
        w2init();
        SB();
        WAITV(11);
      } else WAITV(2 * NG);
      RAWBAR();
      f32x4 acc[NT][4];
      bf16x8 xf0[4], xf1[4], wa0, wa1, wa2, wb0, wb1, wb2;
      int dnext = -2 * 40960;
#define MM_(t, m) do { if constexpr (P == 0) { if constexpr (t == 0) MMW(fi, acc[0][m], wa0, 100, 103, xf0[m]); else if constexpr (t == 1) MMW(fi, acc[1][m], wa1, 104, 107, xf0[m]); else MMW(fi, acc[NT - 1][m], wa2, 108, 111, xf0[m]); } \
    else { if constexpr (t == 0) MMW(fi, acc[0][m], wb0, 112, 115, xf1[m]); else if constexpr (t == 1) MMW(fi, acc[1][m], wb1, 116, 119, xf1[m]); else MMW(fi, acc[NT - 1][m], wb2, 120, 123, xf1[m]); } } while (0)
#define RW_(P, t) do { if constexpr (P == 1) { if constexpr (t == 0) TRW(wa0, 100, 101, 102, 103, wo_[0], 0); else if constexpr (t == 1) TRW(wa1, 104, 105, 106, 107, wo_[1], 0); else TRW(wa2, 108, 109, 110, 111, wo_[NT - 1], 0); } \
    else { if constexpr (t == 0) TRW(wb0, 112, 113, 114, 115, wo_[0], 4096); else if constexpr (t == 1) TRW(wb1, 116, 117, 118, 119, wo_[1], 4096); else TRW(wb2, 120, 121, 122, 123, wo_[NT - 1], 4096); } } while (0)
#define RX_(P, m) do { if constexpr (P == 1) ADSR(xf0[m], xo[0], (m) * 2048); else ADSR(xf1[m], xo[1], (m) * 2048); } while (0)
      auto kstep = [&](auto P_, auto G0, auto GN, auto RD, auto UPD, auto FIRST) {
        constexpr int P = P_, g0 = G0, gn = GN; constexpr bool rd = RD, upd = UPD, fi = FIRST;
        MM_(0, 0); if constexpr (rd) RW_(P, 0);
        MM_(0, 1); if constexpr (rd) RX_(P, 0);
        MM_(0, 2); if constexpr (rd) RX_(P, 1);
        MM_(0, 3); if constexpr (rd) { RX_(P, 2); if constexpr (NT == 2) RX_(P, 3); }
        MM_(1, 0); if constexpr (rd) { if constexpr (NT == 3) RX_(P, 3); else RW_(P, 1); }
        if constexpr (NT == 2 && gn > 0) issue(IC<g0>{});
        MM_(1, 1); if constexpr (rd && NT == 3) RW_(P, 1);
        MM_(1, 2); if constexpr (rd && NT == 3) RW_(P, 2);
        if constexpr (NT == 2 && gn > 1) issue(IC<g0 + 1>{});
        if constexpr (NT == 3 && gn > 0) issue(IC<g0>{});
        MM_(1, 3);
        if constexpr (NT == 3) {
          MM_(2, 0);
          if constexpr (gn > 1) issue(IC<g0 + 1>{});
          MM_(2, 1); MM_(2, 2);
          if constexpr (gn > 2) issue(IC<g0 + 2>{});
          if constexpr (upd) { wo_[0] += dnext; wo_[1] += dnext; wo_[NT - 1] += dnext; xo[0] += dnext; xo[1] += dnext; }
          SB();
          MM_(2, 3);
        } else {
          if constexpr (upd) { wo_[0] += dnext; wo_[1] += dnext; xo[0] += dnext; xo[1] += dnext; }
        }
        SB();
      };
      using BT = std::true_type; using BF = std::false_type;
      RW_(1, 0); RX_(1, 0); RX_(1, 1); RX_(1, 2); RX_(1, 3); RW_(1, 1); if constexpr (NT == 3) RW_(1, 2);
      WAITL(0);
      SB();
      kstep(IC<0>{}, IC<0>{}, IC<0>{}, BT{}, BT{}, BT{});
      if constexpr (ISQ) {
        auto iter = [&](auto G0, auto LAST) {
          constexpr int g0 = G0;
          BAR();
          kstep(IC<1>{}, IC<g0>{}, IC<3>{}, BT{}, BF{}, BF{});
          WAITL(0);
          kstep(IC<0>{}, IC<g0 + 3>{}, IC<2>{}, BT{}, std::integral_constant<bool, !LAST>{}, BF{});
        };
        dnext = 40960;
        WAITV(6); iter(IC<0>{}, BF{});
        WAITV(6); iter(IC<0>{}, BF{});
#pragma unroll 1
        for (int ch = 2; ch < 7; ++ch) { dnext = (ch == 2 || ch == 5) ? -2 * 40960 : 40960; WAITV(5); iter(IC<0>{}, BF{}); }
        WAITV(5); iter(IC<100>{}, BF{});
        WAITV(5); iter(IC<105>{}, BT{});
        BAR();
        kstep(IC<1>{}, IC<110>{}, IC<3>{}, BF{}, BF{}, BF{});
        asm volatile("s_nop 7\n\ts_nop 1" : "+v"(acc[2][0]), "+v"(acc[2][1]), "+v"(acc[2][2]), "+v"(acc[2][3]));
#pragma unroll
        for (int t = 0; t < 3; ++t)
#pragma unroll
          for (int mp = 0; mp < 2; ++mp) {
            PIN(acc[t][2 * mp]); PIN(acc[t][2 * mp + 1]);
            f32x4 a = acc[t][2 * mp], bb = acc[t][2 * mp + 1];
            qp[2 * t + mp] = v4u{pk2(a[0], a[1]), pk2(a[2], a[3]), pk2(bb[0], bb[1]), pk2(bb[2], bb[3])};
          }
      } else {
        unsigned rslot = 0;
#pragma unroll 1
        for (int ch = 0; ch < 7; ++ch) {
          dnext = rslot == 2 ? -2 * 40960 : 40960;
          rslot = rslot == 2 ? 0 : rslot + 1;
          WAITV(NG);
          BAR();
          kstep(IC<1>{}, IC<0>{}, IC<NT>{}, BT{}, BF{}, BF{});
          WAITL(0);
          kstep(IC<0>{}, IC<NT>{}, IC<2>{}, BT{}, BT{}, BF{});
        }
        make_tag();
        dnext = 40960;
        WAITV(NG);
        BAR();
        v4u st[4], cs[4];
        const unsigned sto = WS_RS + (rt128 * 128 + wm2 * 64) * 16, cso = WS_CS + (col * 128 + wn * 32) * 8;
        sfor<4>([&](auto M) { constexpr int m = M; gload16<m * 256>(st[m], p.ws + sto, (unsigned)(r * 16)); });
        kstep(IC<1>{}, IC<0>{}, IC<0>{}, BT{}, BF{}, BF{});
        sfor<4>([&](auto M) { constexpr int m = M; gload16<(m >> 1) * 128 + (m & 1) * 16>(cs[m], p.ws + cso, (unsigned)(g * 32)); });
        WAITL(0);
        kstep(IC<0>{}, IC<100>{}, IC<1>{}, BT{}, BT{}, BF{});
        WAITV(9);
        BAR();
        kstep(IC<1>{}, IC<101>{}, IC<2>{}, BT{}, BF{}, BF{});
        WAITL(0);
        kstep(IC<0>{}, IC<103>{}, IC<1>{}, BT{}, BF{}, BF{});
        BAR();
        kstep(IC<1>{}, IC<104>{}, IC<2>{}, BF{}, BF{}, BF{});
        asm volatile("s_waitcnt vmcnt(6)\n\ts_nop 7" : "+v"(st[0]), "+v"(st[1]), "+v"(st[2]), "+v"(st[3]), "+v"(cs[0]), "+v"(cs[1]), "+v"(cs[2]), "+v"(cs[3])::"memory");
        auto ready = [&]() {
          unsigned bad = 0;
#pragma unroll
          for (int m = 0; m < 4; ++m) bad |= (st[m][1] ^ tag32) | (st[m][3] ^ tag32) | (cs[m][1] ^ tag32) | (cs[m][3] ^ tag32);
          return __all(bad == 0);
        };
        if (__builtin_expect(!ready(), 0)) {
#pragma unroll 1
          for (unsigned spins = 0; spins < (1u << 16); ++spins) {
#pragma unroll
            for (int m = 0; m < 4; ++m) {
              st[m] = __builtin_amdgcn_raw_buffer_load_b128(rs, r * 16 + m * 256, sto, 16);
              cs[m] = __builtin_amdgcn_raw_buffer_load_b128(rs, g * 32 + (m >> 1) * 128 + (m & 1) * 16, cso, 16);
            }
            asm volatile("s_waitcnt vmcnt(0)" ::: "memory");
            if (ready()) break;
          }
        }
        const int hp = 4 * col + wn;
        const int gq = (hp * 13) >> 6;
        const int delta = gq == 0 ? 1 : gq == 1 ? -1 : gq == 2 ? 2 : -2;
        sfor<4>([&](auto M_) {
          constexpr int m = M_;
          const float mean = __uint_as_float(st[m][0]), rstd = __uint_as_float(st[m][2]);
          const float k1 = GC1 * rstd, k2 = GC2 * rstd * rstd * rstd, sd = __builtin_amdgcn_rcpf(rstd);
          int drow = rt128 * 128 + wm2 * 64 + m * 16 + r + delta;
          const bool wrap = (unsigned)drow >= (unsigned)S;
          drow &= (S - 1);
          const unsigned fb = (drow >> 4) * 20 * 1024 + (g * 16 + (drow & 15)) * 16;
          v4u o;
#pragma unroll
          for (int t = 0; t < 2; ++t) {
            pin(acc[t][m]);
#pragma unroll
            for (int h = 0; h < 2; ++h) {
              float h0 = gelu_s(__builtin_fmaf(-mean, __uint_as_float(cs[2 * t + h][0]), acc[t][m][2 * h]), k1, k2, sd);
              float h1 = gelu_s(__builtin_fmaf(-mean, __uint_as_float(cs[2 * t + h][2]), acc[t][m][2 * h + 1]), k1, k2, sd);
              o[2 * t + h] = wrap ? 0u : pk2(h0, h1);
            }
          }
          BSTORE16(o, fb, WS_HS + hp * 1024);
          SB();
        });
        WAITV(0);
        RAWBAR();
        if (tid == 0) set_flag(p.ws, WS_FH + (rt128 * 8 + col) * 128, tag);
        sfor<7>([&](auto I) { w2load(IC<6 + I>{}); });
      }
    };
    if (!isq) s1run(IC<2>{}); else s1run(IC<3>{});
  } else {
    const int z = rt128;
    {
      const int own = rt64 >> 1, nb = (own + ((rt64 & 1) ? 1 : 15)) & 15;
      const int li = lane < 10 ? lane : 0;
      fhoff = WS_FH + (li < 5 ? own * 8 + li : nb * 8 + li - 5) * 128;
    }
    epload();
    v4u selu;
#pragma unroll
    for (int e = 0; e < 4; ++e) selu[e] = r == 2 * e ? 0x3F80u : r == 2 * e + 1 ? 0x3F800000u : 0u;
    const bf16x8 sel = __builtin_bit_cast(bf16x8, selu);
    v4u cr[3][10];
    auto zload = [&](auto K) {
      constexpr int k = K;
      const int task = z + 16 * k < 37 ? z + 16 * k : z;
      const int tp = task < 10 ? 1280 : 3456;
      const char* src = (task < 10 ? (const char*)p.w1 + task * 128 : (const char*)p.w3 + (task - 10) * 128) + (size_t)w * 80 * tp;
      const unsigned vx = (lane >> 3) * tp + (lane & 7) * 16;
      if (k < 2 || z < 5) sfor<10>([&](auto I) { constexpr int i = I; gload16<0>(cr[k][i], src + (size_t)i * 8 * tp, vx); });
      else sfor<10>([&](auto I) {
        constexpr int i = I; gload16<0>(cr[k][i], (const char*)p.w2 + (size_t)(z - 5) * 73728 + i * 8192, l16 + lw); });
    };
    zload(IC<0>{});
    w2init();
    sfor<3>([&](auto K) {
      constexpr int k = K;
      LAS float* cp = (LAS float*)(L + k * 4096);
      if constexpr (k == 1) { zload(IC<2>{}); zload(IC<1>{}); }
      WAITV(0);
      if constexpr (k == 0) make_tag();
      f32x4 c = {0, 0, 0, 0};
#pragma unroll
      for (int i = 0; i < 10; ++i) { pin(cr[k][i]); c = mfma(sel, __builtin_bit_cast(bf16x8, cr[k][i]), c); }
      if (g < 2) *(LAS f32x4*)(cp + (w * 2 + (r >> 3)) * 64 + (r & 7) * 8 + g * 4) = c;
      BAR();
      if (w == 1 + k) {
        f32x2 s = {0, 0};
#pragma unroll
        for (int i = 0; i < 16; ++i) s += *(LAS f32x2*)(cp + i * 64 + (lane & 31) * 2);
        BSTORE16((v4u{__float_as_uint(s[0]), tag32, __float_as_uint(s[1]), tag32}), (lane < 32 && z + 16 * k < 37) ? l16 : OOB, WS_CS + (z + 16 * k) * 512);
      }
    });
    BAR();
    sfor<13>(w2load);
  }
  v4u xr; v2u xs;
  const unsigned orow = (unsigned)(rt64 * 64 + wm * 16 + r) * 1280 + ct * 160;
  const unsigned opair = orow + wk * 64 + (g & 1) * 32 + (g >> 1) * 16;
  const int q4 = (lane >> 2) & 3, pp = lane & 3;
  const char* sb3 = (const char*)p.w3 + (6 * cg + (cg < 12 ? cg : 12)) * 32;
  v4u w3s[7];
  auto w3load = [&](auto N) {
    constexpr int n = N;
    const unsigned dst = n < 4 ? lw + (120 + 8 * n) * 1024 : n > 4 ? lw + (8 * n - 35) * 1024 : (w < 3 ? lw + 152 * 1024 : lw - 3 * 1024);
    if constexpr (n < 11) glds(sb3 + (size_t)(n / 7) * 320 * 3456, v3[n % 7], dst);
    else if constexpr (n < 14) gload16<0>(w3s[n - 11], sb3 + (size_t)320 * 3456, v3[n % 7]);
    else gload16<0>(w3s[n - 11], sb3, v3c[n - 14]);
  };
  const char* sb4 = p.ws + WS_W4 + ct * 370 * 1024 + lw;
  if (w == 7) poll_flags(p.ws, fhoff, tag);
  sfor<4>(w3load);
  WAITV(4);
  gload16<0>(xr, p.x, opair);
  gload8<128>(xs, p.x, orow + g * 8);
  RAWBAR();
  f32x4 acc[5];
  unsigned x1p[3][2];
  v2u jf[4];
  {
    bf16x8 af[10];
    const char* ap = p.ws + WS_HS + (size_t)((rt64 * 4 + wm) * 20 + wk) * 1024;
    sfor<10>([&](auto I) { constexpr int i = I; gload16<(i & 1) * 2048>(af[i], ap + (i >> 1) * 4096, l16); });
    {
      const int e = (lane >= 10) + (lane >= 20) + (lane >= 30);
      const unsigned vo = e * 1120 + l16;
      const int k0 = xcd * 4 + (j >> 3);
      if (lane < 40) {
#pragma unroll
        for (int i = 0; i < 2; ++i) {
          int ks = k0 + 32 * i; if (ks >= 54) ks = 0;
          const int c3 = (ks * 43) >> 7;
          int R = 16 * (6 * c3 + (c3 < 12 ? c3 : 12)) + 32 * (ks - 3 * c3) + (w & 1) * 16 + (w >> 1) * 4;
          if (ks >= 48) R = 224 * (ks - 48) + 96 + (w & 3) * 4 + (w >> 2) * 112;
          glds((const char*)p.w4 + (size_t)R * 1280 + (j & 7) * 160, vo, L_JOB + i * 5120 + w * 640);
        }
        const int ks = k0 < 20 ? k0 : 0;
        const int R = ks < 16 ? 40 * ks + (w & 1) * 20 + (w >> 1) * 4 : 160 * (ks - 16) + 16 + 20 * w;
        glds((const char*)p.wo + (size_t)R * 1280 + (j & 7) * 160, vo, L_JOB + 10240 + w * 640);
      }
    }
    bf16x8 bfr[5];
    const unsigned b0 = wk * 5120 + (4 * g + q4) * 160 + pp * 8, b1 = b0 + 51200;
    sfor<5>([&](auto T) { constexpr int t = T; trb<t, t * 32, t * 32 + 2560>(bfr[t], b0); });
    sfor<10>([&](auto I) {
      constexpr int i = I;
      WAITV_R(9 - i + 3 + (i > 2 ? i - 2 : 0), af[i]);
      if constexpr (i == 5) RAWBAR();
      sfor<5>([&](auto T) {
        constexpr int t = T;
        if constexpr (i < 9) WAITL(8); else WAITL(8 - 2 * t);
        mmb<t, i == 0>(acc[t], bfr[t], af[i]);
        if constexpr (i < 9) { constexpr int o = ((i + 1) % 5) * 10240 + t * 32; trb<t, o, o + 2560>(bfr[t], i + 1 < 5 ? b0 : b1); }
        if constexpr (t == 2 && i >= 2 && i < 9) w3load(IC<(i < 9 ? 9 + i : 11)>{});
      });
    });
    WAITV(7);
    MFMA_FENCE5(acc);
    reduce_wk(L, wm, wk, lane, acc);
    {
      const int f1 = w < 7 ? 8 + w : 14;
      const unsigned ja = L_JOB + g * 1280 + q4 * 160 + pp * 8;
      const unsigned j0 = ja + (w < 5 ? w * 32 : 5120 + (w - 5) * 32), j1 = ja + (f1 < 10 ? 5120 + (f1 - 5) * 32 : 10240 + (f1 - 10) * 32);
      SB();
      ADSTR(jf[0], j0, 0); ADSTR(jf[1], j0, 640); ADSTR(jf[2], j1, 0); ADSTR(jf[3], j1, 640);
    }
    PIN(xr); PIN(xs);
    const v2u xa = sw16(xr[0], xr[2]), xb = sw16(xr[1], xr[3]);
    float sum = 0, sq = 0;
#pragma unroll
    for (int i = 0; i < 3; ++i) {
      const unsigned x0 = i < 2 ? xa[i] : xs[0], x1 = i < 2 ? xb[i] : xs[1];
      x1p[i][0] = pk2(bflo(x0) + acc[i][0], bfhi(x0) + acc[i][1]); x1p[i][1] = pk2(bflo(x1) + acc[i][2], bfhi(x1) + acc[i][3]);
      if (i < 2 || wk == 0) {
        sum = dot2(x1p[i][0], 0x3F803F80u, sum); sum = dot2(x1p[i][1], 0x3F803F80u, sum);
        sq = dot2(x1p[i][0], x1p[i][0], sq); sq = dot2(x1p[i][1], x1p[i][1], sq);
      }
    }
    const unsigned so = WS_X1 + (rt64 * 4 + wm) * 20 * 1024;
    BSTORE16((v4u{x1p[0][0], x1p[0][1], x1p[1][0], x1p[1][1]}), l16, so + (2 * ct + wk) * 1024);
    const v2u s0 = sw16(x1p[2][0], x1p[2][0]), s1 = sw16(x1p[2][1], x1p[2][1]);
    BSTORE16((v4u{s0[0], s1[0], s0[1], s1[1]}), (wk == 0 && !(g & 1)) ? (g >> 1) * 256 + r * 16 : OOB, so + 16 * 1024 + ct * 512);
    sum = xsum4(sum); sq = xsum4(sq);
    __builtin_amdgcn_raw_buffer_store_b64(__builtin_bit_cast(v2u, f32x2{sum, sq}), rs, g == 0 ? r * 8 : OOB, WS_ST + ((rt64 * 4 + wm) * 16 + ct * 2 + wk) * 128, 16);
  }
  {
    asm volatile("s_waitcnt lgkmcnt(0)" : "+v"(jf[0]), "+v"(jf[1]), "+v"(jf[2]), "+v"(jf[3])::"memory");
    const int k0 = xcd * 4 + (j >> 3), f1 = w < 7 ? 8 + w : 14;
    const unsigned d0 = WS_W4 + ((j & 7) * 74 + k0) * 5120;
    BSTORE16((v4u{jf[0][0], jf[0][1], jf[1][0], jf[1][1]}), (w < 5 || k0 < 22) ? l16 : OOB, d0 + (w < 5 ? w * 1024 : 32 * 5120 + (w - 5) * 1024));
    BSTORE16((v4u{jf[2][0], jf[2][1], jf[3][0], jf[3][1]}), (f1 < 10 ? k0 < 22 : (k0 < 20 && w < 7)) ? l16 : OOB, d0 + (f1 < 10 ? 32 * 5120 + (f1 - 5) * 1024 : 54 * 5120 + (f1 - 10) * 1024));
    WAITV(0);
    RAWBAR();
    if (tid < 2) set_flag(p.ws, tid == 0 ? WS_F2 + (rt64 * 8 + ct) * 128 : WS_FW + b * 8, tag);
  }
  sfor<7>([&](auto I) { w3load(IC<I + 4>{}); });
  sfor<7>([&](auto I) { constexpr int n = 11 + I; dsw16<(n - 11) * 8192>(l16 + 53 * 1024 + (n == 17 ? (w & 3) * 1024 : lw), w3s[I]); });
  {
    if (w == 6) {
      const unsigned co = WS_CS + (640 + (6 * cg + (cg < 12 ? cg : 12)) * 16) * 8, cv_ = (lane < (cg < 12 ? 56 : 48) ? lane : 0) * 16;
      v4u c;
#pragma unroll 1
      for (unsigned spins = 0; spins < (1u << 16); ++spins) {
        asm volatile("global_load_dwordx4 %0, %1, %2 sc1\n\ts_waitcnt vmcnt(0)" : "=v"(c) : "v"(cv_), "s"(p.ws + co) : "memory");
        if (__all(c[1] == tag32 && c[3] == tag32)) break;
      }
      *(LAS v2u*)(L + L_CS3 + lane * 8) = v2u{c[0], c[2]};
    }
    if (w == 7) poll_flags(p.ws, WS_F2 + (rt128 * 16 + r) * 128, tag);
    WAITV(0);
    BAR();
    const int rt = rt128 * 8 + w;
    constexpr int PD = 10;
    bf16x8 af[20];
    const char* ap = p.ws + WS_X1 + (size_t)rt * 20 * 1024;
    sfor<PD>([&](auto I) { constexpr int i = I; gload16<(i & 3) * 1024>(af[i], ap + (i >> 2) * 4096, l16); });
    f32x4 acc3[7]; f32x2 stp[4];
    bf16x8 bfr[7];
    const unsigned c1 = (4 * g + q4) * 224 + pp * 8, c0 = c1 + 122880, c2 = c1 + 57344;
    sfor<7>([&](auto T) { constexpr int t = T; trb<t, t * 32, t * 32 + 3584>(bfr[t], c0); });
    u64 fwv, fqv;
    const unsigned fwoff = WS_FW + (w * 32 + (lane & 31)) * 8;
    unsigned fqoff;
    {
      const int own = rt64 >> 1, pv = ((rt64 & 1) || own == 0) ? own : own - 1;
      fqoff = WS_FQ + (lane < 10 ? own * 128 + lane * 8 : lane < 20 ? pv * 128 + (lane - 10) * 8 : own * 128);
    }
    const unsigned qso = WS_QKV + ((rt128 * 4 + (w & 1) * 2) * 120 + (5 * half + sidx) * 12 + (w >> 1) * 3) * 1024;
    int art = 2 * rt64 + (w < 2 ? w : w < 5 ? w - 3 : w - 6); if (art < 0) art = 0;
    const char* asb = p.ws + WS_QKV + (size_t)(art * 120 + (w < 2 ? 0 : w < 5 ? 40 : 80) + 5 * ct) * 1024;
    sfor<20>([&](auto I) {
      constexpr int ks = I;
      constexpr auto nA = [](int s) { return s <= 17 ? 1 : 0; };
      constexpr auto nB = [](int s) { return (s == 7 || s >= 9) ? 1 : 0; };
      constexpr int cnt = [&]() {
        int n = 0;
        if (ks < PD) { n = PD - 1 - ks; for (int s = 0; s < ks; ++s) n += nA(s) + nB(s); }
        else { int s0 = ks - PD; n = nB(s0); for (int s = s0 + 1; s < ks; ++s) n += nA(s) + nB(s); }
        return n; }();
      if constexpr (ks == 9) {
        WAITV_R(1, fwv);
        if (!__all(fwv == tag)) poll_flags(p.ws, fwoff, tag);
      }
      WAITV_R(cnt, af[ks]);
      if constexpr (ks == 9 || ks == 12 || ks == 15) RAWBAR();
      if constexpr (ks == 9) { if (tid == 0 && role == 0) set_flag(p.ws, WS_FQ + rt128 * 128 + (5 * half + sidx) * 8, tag); }
      sfor<7>([&](auto T) {
        constexpr int t = T;
        if constexpr (ks < 19) WAITL(12); else WAITL(12 - 2 * t);
        mmb<t, ks == 0>(acc3[t], bfr[t], af[ks]);
        if constexpr (ks < 19) { constexpr int k1 = ks + 1, o = (k1 < 5 ? k1 : k1 < 13 ? k1 - 5 : k1 - 13) * 7168 + t * 32; trb<t, o, o + 3584>(bfr[t], k1 < 5 ? c0 : k1 < 13 ? c1 : c2); }
        if constexpr (t == 1) {
          if constexpr (ks < 10) gload16<((ks + PD) & 3) * 1024>(af[ks + PD], ap + ((ks + PD) >> 2) * 4096, l16);
          else if constexpr (ks < 14) gload8<(ks - 10) * 128>(stp[ks - 10], p.ws + WS_ST + (size_t)rt * 2048, (unsigned)(g * 512 + r * 8));
          else if constexpr (ks < 17) { constexpr int f = 40 + 16 * (ks - 14); bload<f, B0 + f>(sb4, l16, lw); }
          else if constexpr (ks == 17) gload8<0>(fqv, p.ws, fqoff);
        }
        if constexpr (t == 4) {
          if constexpr (ks == 7) gload8<0>(fwv, p.ws, fwoff);
          else if constexpr (ks >= 9) {
            constexpr int fs[11] = {0, 8, 16, 24, 32, 48, 64, 80, 88, 96, 102};
            bload<fs[ks - 9], B0 + fs[ks - 9]>(sb4, l16, lw);
          }
        }
        if constexpr (t == 6 && ks < 6) { if (role == 0) BSTORE16(qp[ks], l16, qso + (ks >> 1) * 1024 + (ks & 1) * 120 * 1024); }
      });
    });
    WAITV_R(3, fqv);
    if (!__all(fqv == tag)) poll_flags(p.ws, fqoff, tag);
    f32x4 cv[7];
    const unsigned cva = L_CS3 + g * 16;
    sfor<7>([&](auto T) { constexpr int t = T; dsr16<t * 64>(cv[t], cva); });
    asm volatile("s_barrier\n\ts_nop 7" : "+v"(acc3[0]), "+v"(acc3[1]), "+v"(acc3[2]), "+v"(acc3[3]), "+v"(acc3[4]), "+v"(acc3[5]), "+v"(acc3[6]), "+v"(stp[0]), "+v"(stp[1]), "+v"(stp[2]), "+v"(stp[3])::"memory");
    glds(asb, l16, L_ATT + w * 5120); glds_same_m0<1024>(asb, l16); glds_same_m0<2048>(asb, l16); glds_same_m0<3072>(asb, l16);
    glds(asb + 4096, l16, L_ATT + w * 5120 + 4096);
    const float sum = xsum4((stp[0][0] + stp[1][0]) + (stp[2][0] + stp[3][0]));
    const float sq = xsum4((stp[0][1] + stp[1][1]) + (stp[2][1] + stp[3][1]));
    const float mean = sum * (1.0f / D);
    const float var = fmaxf(sq * (1.0f / D) - mean * mean, 0.f) + 1e-5f;
    const float rstd = __builtin_amdgcn_rsqf(var), sd = var * rstd;
    const float k1 = GC1 * rstd, k2 = GC2 * rstd * rstd * rstd;
    asm volatile("s_waitcnt lgkmcnt(0)" : "+v"(cv[0]), "+v"(cv[1]), "+v"(cv[2]), "+v"(cv[3]), "+v"(cv[4]), "+v"(cv[5]), "+v"(cv[6]));
    const unsigned so = WS_U + rt * 54 * 1024;
    sfor<4>([&](auto PR) {
      constexpr int pr = PR;
      v4u o;
#pragma unroll
      for (int tt = 0; tt < (pr < 3 ? 2 : 1); ++tt) {
        const int t = 2 * pr + tt;
        float h[4];
#pragma unroll
        for (int e = 0; e < 4; ++e) h[e] = gelu_s(__builtin_fmaf(-mean, cv[t][e], acc3[t][e]), k1, k2, sd);
        o[2 * tt] = pk2(h[0], h[1]); o[2 * tt + 1] = pk2(h[2], h[3]);
      }
      if constexpr (pr < 3) BSTORE16(o, l16, so + (3 * cg + pr) * 1024);
      else {
        const v2u s0 = sw16(o[0], o[0]), s1 = sw16(o[1], o[1]);
        BSTORE16((v4u{s0[0], s1[0], s0[1], s1[1]}), (cg < 12 && !(g & 1)) ? (g >> 1) * 256 + r * 16 : OOB, so + 48 * 1024 + cg * 512);
      }
      SB();
    });
  }
#define SMOD 2
  constexpr int NSTG = 28 - 28 / SMOD;
  v4u stg[NSTG];
  auto stage = [&](auto Q) { constexpr int k = Q, q = k + k / (SMOD - 1); gload16<0>(stg[k], sb4 + (size_t)(q < 27 ? 150 + 8 * q : 362) * 1024, l16); };
  WAITV(4);
  RAWBAR();
  {
    const unsigned offc = (wm >> 1) * 5120 + (wm & 1) * 8;
    const unsigned offp = ((wm + 1) >> 1) * 5120 + ((wm + 1) & 1) * 8;
    auto qaddr = [&](int Q, int row) { return (unsigned)(L_ATT + (Q >> 2) * 1024 + ((Q & 3) * 16 + row) * 16); };
    bf16x4 ql[2], qh[2], kpl[2], kph[2], kcl[2], kch[2], vp0[2], vc0[2], vp1[2], vc1[2];
#pragma unroll
    for (int h = 0; h < 2; ++h) {
      const int hh = 2 * wk + h;
      const unsigned a0 = qaddr(5 * hh + g, r), a1 = qaddr(5 * hh + 4, r);
      const unsigned v0 = qaddr(5 * hh + pp, 4 * g + q4), v1 = qaddr(5 * hh + 4, 4 * g + q4);
      ADSR8(ql[h], a0 + offc, 0); ADSR8(qh[h], a1 + offc, 0);
      ADSR8(kcl[h], a0 + offc, 15360); ADSR8(kch[h], a1 + offc, 15360);
      ADSR8(kpl[h], a0 + offp, 10240); ADSR8(kph[h], a1 + offp, 10240);
      ADSTR(vc0[h], v0 + offc, 30720); ADSTR(vc1[h], v1 + offc, 30720);
      ADSTR(vp0[h], v0 + offp, 25600); ADSTR(vp1[h], v1 + offp, 25600);
    }
    asm volatile("s_waitcnt lgkmcnt(0)" : "+v"(ql[0]), "+v"(qh[0]), "+v"(kcl[0]), "+v"(kch[0]), "+v"(kpl[0]), "+v"(kph[0]), "+v"(vc0[0]), "+v"(vc1[0]), "+v"(vp0[0]), "+v"(vp1[0]),
                 "+v"(ql[1]), "+v"(qh[1]), "+v"(kcl[1]), "+v"(kch[1]), "+v"(kpl[1]), "+v"(kph[1]), "+v"(vc0[1]), "+v"(vc1[1]), "+v"(vp0[1]), "+v"(vp1[1])::"memory");
    const int dd = r - 4 * g;
    const bool prev_ok = (rt64 | wm) != 0;
    unsigned o0p[2][2], o1p[2][2];
#pragma unroll
    for (int h = 0; h < 2; ++h) {
      bf16x4 qhh = qh[h];
      if (g != 0) qhh = bf16x4{0, 0, 0, 0};
      const bf16x8 qf = cat(ql[h], qhh);
      const f32x4 z = {0, 0, 0, 0};
      f32x4 sp = mfma(cat(kpl[h], kph[h]), qf, z), sc = mfma(cat(kcl[h], kch[h]), qf, z);
      const float NEG = -__builtin_inff();
      float mx = NEG;
#pragma unroll
      for (int e = 0; e < 4; ++e) {
        const int dc = dd - e;
        const bool even = (dc & 1) == 0;
        const bool vc = even && dc >= 0, vp = even && dc < 0 && prev_ok;
        sp[e] = vp ? sp[e] : NEG; sc[e] = vc ? sc[e] : NEG;
        mx = fmaxf(mx, fmaxf(sp[e], sc[e]));
      }
      mx = xmax4(mx);
      const float cexp = 0.22360679774997896f * 1.4426950408889634f;
      const float mc = mx * cexp;
      float sum = 0;
#pragma unroll
      for (int e = 0; e < 4; ++e) {
        sp[e] = __builtin_amdgcn_exp2f(__builtin_fmaf(sp[e], cexp, -mc)); sc[e] = __builtin_amdgcn_exp2f(__builtin_fmaf(sc[e], cexp, -mc));
        sum += sp[e] + sc[e];
      }
      sum = xsum4(sum);
      const v4u pu = {pk2(sp[0], sp[1]), pk2(sp[2], sp[3]), pk2(sc[0], sc[1]), pk2(sc[2], sc[3])};
      const bf16x8 pf = __builtin_bit_cast(bf16x8, pu);
      const f32x4 o0 = mfma(cat(vp0[h], vc0[h]), pf, z), o1 = mfma(cat(vp1[h], vc1[h]), pf, z);
      const float inv = __builtin_amdgcn_rcpf(sum);
      o0p[h][0] = pk2(o0[0] * inv, o0[1] * inv); o0p[h][1] = pk2(o0[2] * inv, o0[3] * inv);
      o1p[h][0] = pk2(o1[0] * inv, o1[1] * inv); o1p[h][1] = pk2(o1[2] * inv, o1[3] * inv);
      if (h == 0) {
        SB();
        WAITV(0);
        RAWBAR();
        if (tid == 0) set_flag(p.ws, WS_F3 + (rt128 * 16 + cg) * 128, tag);
        sfor<5>([&](auto I) { constexpr int f = 110 + 8 * I; bload<f, B0 + f>(sb4, l16, lw); });
        sfor<NSTG / 2>(stage);
        SB();
      }
    }
    const unsigned so = WS_O + (rt64 * 4 + wm) * 20 * 1024;
    BSTORE16((v4u{o0p[0][0], o0p[0][1], o0p[1][0], o0p[1][1]}), l16, so + (2 * ct + wk) * 1024);
    BSTORE16((v4u{o1p[0][0], o1p[0][1], o1p[1][0], o1p[1][1]}), g == 0 ? l16 : OOB, so + 16 * 1024 + ct * 512 + wk * 256);
  }
  {
    if (w == 7) poll_flags(p.ws, WS_F3 + ((rt64 >> 1) * 16 + r) * 128, tag);
    sfor<NSTG - NSTG / 2>([&](auto I) { stage(IC<NSTG / 2 + I>{}); });
    if (b == 0 && tid == 0) set_flag(p.ws, WS_EP, tag);
    WAITV(NSTG + 2);
    RAWBAR();
    const unsigned bA0 = l16 + wk * 5120, bA1 = bA0 + 61440, bA2 = bA0 + 122880;
#define BRD(dst, s) do { if constexpr ((s) < 60) ADSR(dst, bA0, (s) * 1024); else if constexpr ((s) < 120) ADSR(dst, bA1, ((s) - 60) * 1024); else ADSR(dst, bA2, ((s) - 120) * 1024); } while (0)
    const unsigned wA0 = l16 + lw, wA1 = wA0 + 61440, wA2 = wA0 + 122880;
    constexpr int PD = 10, NS = 37;
    bf16x8 af[NS];
    f32x4 acco[5];
    float gate; u64 fav;
    const unsigned faoff = WS_FA + (rt64 * 8 + (lane & 7)) * 128;
    const char* ap = p.ws + WS_U + (size_t)((rt64 * 4 + wm) * 54 + wk) * 1024;
    const char* op = p.ws + WS_O + (size_t)((rt64 * 4 + wm) * 20 + wk) * 1024;
    sfor<PD>([&](auto I) { constexpr int i = I; gload16<(i & 1) * 2048>(af[i], ap + (i >> 1) * 4096, l16); });
    bf16x8 bfr[5];
    sfor<5>([&](auto T) { constexpr int t = T; BRD(bfr[t], B0 + t); });
    sfor<NS>([&](auto I) {
      constexpr int i = I;
      constexpr auto nA = [](int s) { return (s + PD <= NS ? 1 : 0) + (s == 12 ? 1 : 0); };
      constexpr auto gB = [](int s) { int n = s < 2 ? 0 : (s - 2) + (s - 2) / 4; return n > 28 ? 28 : n; };
      constexpr auto nG = [=](int s) { return gB(s + 1) - gB(s); };
      constexpr auto nB = [=](int s) { int n = 0; for (int q = gB(s); q < gB(s + 1); ++q) n += q % SMOD == SMOD - 1; return n; };
      constexpr int cntA = [&]() {
        int n = 0;
        if (i < PD) { n = PD - 1 - i; for (int s = 0; s < i; ++s) n += nA(s) + nB(s); }
        else { int s0 = i - PD; n = nB(s0) + (s0 == 12 ? 1 : 0); for (int s = s0 + 1; s < i; ++s) n += nA(s) + nB(s); }
        return n; }();
      constexpr int cntB = [&]() {
        if (10 * (i + 3) <= 150) return 63;
        int q = (10 * (i + 3) - 150 + 7) / 8 - 1; if (q > 27) q = 27;
        int s0 = 2; while (gB(s0 + 1) <= q) ++s0;
        int n = 0;
        for (int s = s0 + 1; s < i; ++s) n += nA(s) + nB(s);
        return n; }();
      constexpr bool bar = (i & 1) == 0 && i >= 2 && i <= 34;
      if constexpr (i == 17) {
        constexpr int c = nB(12) + nA(13) + nB(13) + nA(14) + nB(14) + nA(15) + nB(15) + nA(16) + nB(16);
        WAITV_R(c, fav);
        if (!__all(fav == tag)) poll_flags(p.ws, faoff, tag);
      }
      WAITV_R((bar && cntB < cntA) ? cntB : cntA, af[i]);
      if constexpr (bar) RAWBAR();
      if constexpr (i == 2) { if (tid == 0) set_flag(p.ws, WS_FA + (rt64 * 8 + ct) * 128, tag); }
      sfor<5>([&](auto T) {
        constexpr int t = T;
        if constexpr (i < NS - 1) WAITL(4); else WAITL(4 - t);
        if constexpr (i == 0) AMFMA0(acc[t], bfr[t], af[i]); else if constexpr (i < 27) AMFMA(acc[t], bfr[t], af[i]);
        else if constexpr (i == 27) AMFMA0(acco[t], bfr[t], af[i]); else AMFMA(acco[t], bfr[t], af[i]);
        if constexpr (i < NS - 1) BRD(bfr[t], (B0 + 10 * (i + 1)) % RING + t);
        if constexpr (t == 0) {
          constexpr int n = i + PD;
          if constexpr (n < 27) gload16<(n & 1) * 2048>(af[n], ap + (n >> 1) * 4096, l16);
          else if constexpr (n < NS) gload16<((n - 27) & 1) * 2048>(af[n], op + ((n - 27) >> 1) * 4096, l16);
          else if constexpr (n == NS) gload4<0>(gate, p.ws + WS_GT + (rt64 * 64 + wm * 16) * 16, (unsigned)(r * 16));
        }
        if constexpr (t == 1 && i == 12) gload8<0>(fav, p.ws, faoff);
        if constexpr ((t == 2 && nG(i) >= 1) || (t == 4 && nG(i) == 2)) {
          constexpr int q = gB(i) + (t == 4 ? 1 : 0); constexpr int f = q < 27 ? 150 + 8 * q : 362;
          if constexpr (q % SMOD != SMOD - 1) {
            constexpr int sl = (B0 + f) % RING, k = q - q / SMOD;
            if constexpr (sl + 7 < RING) dsw16<(sl % 60) * 1024>(sl < 60 ? wA0 : sl < 120 ? wA1 : wA2, stg[k]);
            else dsw16<0>(l16 + (sl + w >= RING ? sl + w - RING : sl + w) * 1024, stg[k]);
          } else bload<f, B0 + f>(sb4, l16, lw);
        }
      });
    });
    WAITV_R(0, gate);
    MFMA_FENCE5(acc);
    MFMA_FENCE5(acco);
    const float ng = 1.f - gate;
#pragma unroll
    for (int t = 0; t < 5; ++t) acc[t] = gate * acco[t] + ng * acc[t];
    reduce_wk(L + 90 * 1024, wm, wk, lane, acc);
    unsigned o_[3][2];
#pragma unroll
    for (int i = 0; i < 3; ++i) {
      o_[i][0] = pk2(__builtin_fmaf(ng, bflo(x1p[i][0]), acc[i][0]), __builtin_fmaf(ng, bfhi(x1p[i][0]), acc[i][1]));
      o_[i][1] = pk2(__builtin_fmaf(ng, bflo(x1p[i][1]), acc[i][2]), __builtin_fmaf(ng, bfhi(x1p[i][1]), acc[i][3]));
    }
    const __amdgpu_buffer_rsrc_t ro = __builtin_amdgcn_make_buffer_rsrc((void*)p.out, 0, S * D * 2, 0x00020000);
    const v2u a0 = sw16(o_[0][0], o_[1][0]), a1 = sw16(o_[0][1], o_[1][1]), s0 = sw16(o_[2][0], o_[2][0]), s1 = sw16(o_[2][1], o_[2][1]);
    __builtin_amdgcn_raw_buffer_store_b128(v4u{a0[0], a1[0], a0[1], a1[1]}, ro, opair, 0, 16);
    __builtin_amdgcn_raw_buffer_store_b128(v4u{s0[0], s1[0], s0[1], s1[1]}, ro, (wk == 0 && !(g & 1)) ? orow + 128 + (g >> 1) * 16 : OOB, 0, 16);
  }
}

extern "C" void kernel_launch(void* const* d_in, const int* in_sizes, int n_in, void* d_out, int out_size, void* d_ws, size_t ws_size, hipStream_t stream) {
  Params p;
  p.x = (const u16*)d_in[0]; p.wqkv = (const u16*)d_in[1]; p.wo = (const u16*)d_in[2];
  p.w1 = (const u16*)d_in[4]; p.w2 = (const u16*)d_in[5]; p.w3 = (const u16*)d_in[6]; p.w4 = (const u16*)d_in[7]; p.gw = (const u16*)d_in[8];
  p.out = (u16*)d_out; p.ws = (char*)d_ws;
  const int lds = 163840;
  (void)hipFuncSetAttribute((const void*)mega, hipFuncAttributeMaxDynamicSharedMemorySize, lds);
  mega<<<256, 512, lds, stream>>>(p);
}
```
